```python
import torch

def _softmax(x, axis=-1):
    return torch.softmax(x, dim=axis)
def _gelu(x):
    return torch.nn.functional.gelu(x, approximate="tanh")
def _silu(x):
    return torch.nn.functional.silu(x)
def _sigmoid(x):
    return torch.sigmoid(x)
def _layernorm(x, axis=-1, eps=1e-5):
    m = x.mean(dim=axis, keepdim=True)
    v = x.var(dim=axis, keepdim=True, unbiased=False)
    return (x - m) / torch.sqrt(v + eps)
def _scatter_add(tgt, idx, src, axis=0):
    return tgt.clone().index_add_(axis, idx, src)
def _segment_sum(src, seg, n):
    out = torch.zeros((n,) + src.shape[1:], dtype=src.dtype)
    return out.index_add_(0, seg, src)
def _pad(x, pw):
    flat = tuple(v for pair in reversed(pw) for v in pair)
    return torch.nn.functional.pad(x, flat)

kernel_name = 'concat_heads__spectral_gfnet__ssm_conv_s5__prompt'

def setup_inputs():

    _w = (lambda a: (lambda t: t.to(torch.bfloat16) if t.is_floating_point() else t)(torch.as_tensor(a)))
    def _A_setup():
        B, S, D = 1, 2048, 1024
        Df = D // 2 + 1
        return {
            "x": _w((lambda *s: torch.randn(*s, dtype=torch.bfloat16) * 0.05)(B, S, D)),
            "w_r": _w((lambda *s: torch.randn(*s, dtype=torch.bfloat16) * 0.8)(S, Df) * 0.02),
            "w_i": _w((lambda *s: torch.randn(*s, dtype=torch.bfloat16) * 0.8)(S, Df) * 0.02),
        }
    _A_in = _A_setup()

    def _B_setup():
        B, S, D = 1, 2048, 1024
        P = D
        Lambda_re = -0.5 * (lambda _x: torch.exp(torch.as_tensor(_x)))((lambda *s: torch.randn(*s, dtype=torch.bfloat16) * 0.2)(P))
        Lambda_im = (lambda *s: torch.randn(*s, dtype=torch.bfloat16) * 0.4)(P) * 1.0
        B_re = (lambda *s: torch.randn(*s, dtype=torch.bfloat16) * 0.2)(P, D) * (D ** -0.5)
        B_im = (lambda *s: torch.randn(*s, dtype=torch.bfloat16) * 0.2)(P, D) * (D ** -0.5)
        C_re = (lambda *s: torch.randn(*s, dtype=torch.bfloat16) * 0.2)(D, P) * (P ** -0.5)
        C_im = (lambda *s: torch.randn(*s, dtype=torch.bfloat16) * 0.2)(D, P) * (P ** -0.5)
        log_dt = (lambda *s: torch.randn(*s, dtype=torch.bfloat16) * 0.2)(P) * 0.3 - 2.0
        Dskip = (lambda *s: torch.randn(*s, dtype=torch.bfloat16) * 0.05)(D)
        return {
            "x": _w((lambda *s: torch.randn(*s, dtype=torch.bfloat16) * 0.05)(B, S, D)),
            "Lambda_re": _w(Lambda_re),
            "Lambda_im": _w(Lambda_im),
            "B_re": _w(B_re),
            "B_im": _w(B_im),
            "C_re": _w(C_re),
            "C_im": _w(C_im),
            "log_dt": _w(log_dt),
            "Dskip": _w(Dskip),
        }
    _B_in = _B_setup()

    return {
        "A_x": _A_in["x"],
        "A_w_r": _A_in["w_r"],
        "A_w_i": _A_in["w_i"],
        "B_x": _B_in["x"],
        "B_Lambda_re": _B_in["Lambda_re"],
        "B_Lambda_im": _B_in["Lambda_im"],
        "B_B_re": _B_in["B_re"],
        "B_B_im": _B_in["B_im"],
        "B_C_re": _B_in["C_re"],
        "B_C_im": _B_in["C_im"],
        "B_log_dt": _B_in["log_dt"],
        "B_Dskip": _B_in["Dskip"],
    }

def reference(A_x, A_w_r, A_w_i, B_x, B_Lambda_re, B_Lambda_im, B_B_re, B_B_im, B_C_re, B_C_im, B_log_dt, B_Dskip):
    def _A_ref(x, w_r, w_i):
        B, S, D = x.shape
        X = (lambda x, axes=(-2,-1): torch.fft.rfft2(x.float(), dim=axes))(x, axes=(-2, -1))
        W = w_r + 1j * w_i
        X = X * W
        y = (lambda x, s, axes=(-2,-1): torch.fft.irfft2(x, s=s, dim=axes).to(torch.bfloat16))(X, (S, D), axes=(-2, -1))
        return y

    def _B_ref(x, Lambda_re, Lambda_im, B_re, B_im, C_re, C_im, log_dt, Dskip):
        Bsz, L, H = x.shape
        Lam = Lambda_re + 1j * Lambda_im
        Bmat = B_re + 1j * B_im
        Cmat = C_re + 1j * C_im
        dt = torch.exp(log_dt)
        Lam_bar = torch.exp(dt * Lam)
        B_bar = ((Lam_bar - 1.0) / Lam)[:, None] * Bmat
        Bu = torch.einsum("bld,pd->blp", x + 0j, B_bar)
        h = torch.zeros_like(Bu[:, 0, :])
        hs = []
        for t in range(L):
            h = Lam_bar[None, :] * h + Bu[:, t, :]
            hs.append(h)
        hseq = torch.stack(hs, 1)
        y = 2.0 * torch.einsum("blp,dp->bld", hseq, Cmat).real + x * Dskip[None, None, :]
        return y

    a = _A_ref(A_x, A_w_r, A_w_i)
    b = _B_ref(B_x, B_Lambda_re, B_Lambda_im, B_B_re, B_B_im, B_C_re, B_C_im, B_log_dt, B_Dskip)
    return torch.concatenate([a, b], axis=-1)
```

```cpp
#include <hip/hip_runtime.h>
#include <stdint.h>

namespace {

#if defined(__HIP_DEVICE_COMPILE__) && __HIP_DEVICE_COMPILE__
#pragma clang attribute push(__attribute__((target("no-packed-fp32-ops"))), apply_to = function)
#endif
__device__ __forceinline__ int tid_x() { return __builtin_amdgcn_workitem_id_x(); }
__device__ __forceinline__ int bid_x() { return __builtin_amdgcn_workgroup_id_x(); }

typedef float cf __attribute__((ext_vector_type(2)));
typedef short bf16x8 __attribute__((ext_vector_type(8)));
typedef short s16x4 __attribute__((ext_vector_type(4)));
typedef _Float16 f16x8 __attribute__((ext_vector_type(8)));
typedef _Float16 f16x2 __attribute__((ext_vector_type(2)));
typedef __bf16 b16x2 __attribute__((ext_vector_type(2)));
typedef float f32x4 __attribute__((ext_vector_type(4)));
typedef uint32_t u32x4 __attribute__((ext_vector_type(4)));
typedef uint32_t u32x2 __attribute__((ext_vector_type(2)));
typedef unsigned short u16;
typedef unsigned long long u64;
typedef __attribute__((address_space(3))) uint32_t lds_u32;

__device__ __forceinline__ float u2f(uint32_t u) { return __builtin_bit_cast(float, u); }
__device__ __forceinline__ uint32_t f2u(float f) { return __builtin_bit_cast(uint32_t, f); }
__device__ __forceinline__ float bf2f(u16 b) { return u2f((uint32_t)b << 16); }
__device__ __forceinline__ float rbf(float f) { return bf2f(__builtin_bit_cast(u16, (__bf16)f)); }
__device__ __forceinline__ uint32_t pk_bf(float a, float b) {
  const b16x2 h = b16x2{(__bf16)a, (__bf16)b};
  return __builtin_bit_cast(uint32_t, h);
}
__device__ __forceinline__ uint32_t pk_h(float a, float b) {
  const f16x2 h = f16x2{(_Float16)a, (_Float16)b};
  return __builtin_bit_cast(uint32_t, h);
}
__device__ __forceinline__ cf fin(cf v) {
  float x = v.x, y = v.y;
  asm("" : "+v"(x), "+v"(y));
  return cf{x, y};
}
__device__ __forceinline__ cf cmul(cf a, cf b) {
  return cf{__builtin_fmaf(a.x, b.x, -(a.y * b.y)), __builtin_fmaf(a.x, b.y, a.y * b.x)};
}
__device__ __forceinline__ cf cmad(cf a, cf b, cf c) {
  return cf{__builtin_fmaf(a.x, b.x, __builtin_fmaf(-a.y, b.y, c.x)), __builtin_fmaf(a.x, b.y, __builtin_fmaf(a.y, b.x, c.y))};
}
__device__ __forceinline__ cf cisr(int k, float inv_n) {
  const float r = (float)k * inv_n;
  return cf{__builtin_amdgcn_cosf(r), -__builtin_amdgcn_sinf(r)};
}
template <int N> __device__ __forceinline__ cf cis(int k) { return cisr(k, 1.0f / N); }
template <int N> __device__ __forceinline__ void wait_vm() { asm volatile("s_waitcnt vmcnt(%0)" ::"n"(N) : "memory"); }
__device__ __forceinline__ void wait_lgkm0() { asm volatile("s_waitcnt lgkmcnt(0)" ::: "memory"); }
__device__ __forceinline__ void glds2_dev(const void* sb, uint32_t v0, uint32_t lds_dst) {
  uint32_t keep;
  asm volatile(
      "s_mov_b32 %0, m0\n\ts_mov_b32 m0, %3\n\ts_nop 0\n\tglobal_load_lds_dwordx4 %1, %2 sc1\n\t"
      "global_load_lds_dwordx4 %1, %2 offset:1024 sc1\n\ts_mov_b32 m0, %0"
      : "=&s"(keep)
      : "v"(v0), "s"(sb), "s"(lds_dst)
      : "memory");
}
__device__ __forceinline__ void gdev2(const void* sb, uint32_t v0, uint32_t v1, uint32_t lds_dst) {
  uint32_t keep;
  asm volatile(
      "s_mov_b32 %0, m0\n\ts_mov_b32 m0, %4\n\ts_nop 0\n\tglobal_load_lds_dwordx4 %1, %3 sc1\n\t"
      "global_load_lds_dwordx4 %2, %3 offset:1024 sc1\n\ts_mov_b32 m0, %0"
      : "=&s"(keep)
      : "v"(v0), "v"(v1), "s"(sb), "s"(lds_dst)
      : "memory");
}
__device__ __forceinline__ void glds4(const void* sb0, const void* sb1, uint32_t v0, uint32_t v1, uint32_t v2, uint32_t v3,
                                      uint32_t lds_dst) {
  uint32_t keep;
  asm volatile(
      "s_mov_b32 %0, m0\n\ts_mov_b32 m0, %7\n\ts_nop 0\n\tglobal_load_lds_dwordx4 %1, %5\n\t"
      "global_load_lds_dwordx4 %2, %5 offset:1024\n\tglobal_load_lds_dwordx4 %3, %6 offset:2048\n\t"
      "global_load_lds_dwordx4 %4, %6 offset:3072\n\ts_mov_b32 m0, %0"
      : "=&s"(keep)
      : "v"(v0), "v"(v1), "v"(v2), "v"(v3), "s"(sb0), "s"(sb1), "s"(lds_dst)
      : "memory");
}
__device__ __forceinline__ constexpr uint32_t lds_base(char*) { return 256; }
__device__ __forceinline__ void bar() {
  __builtin_amdgcn_sched_barrier(0);
  asm volatile("" ::: "memory");
  __builtin_amdgcn_s_barrier();
  asm volatile("" ::: "memory");
  __builtin_amdgcn_sched_barrier(0);
}
__device__ __forceinline__ void bar_lds() {
  asm volatile("s_waitcnt lgkmcnt(0)" ::: "memory");
  bar();
}
template <int W> __device__ __forceinline__ void lds_signal(uint32_t U) {
  asm volatile("s_mov_b64 exec, 1\n\tds_add_u32 %0, %0 offset:%1\n\ts_mov_b64 exec, -1" ::"v"(U), "n"(W * 4 - 4) : "memory");
}
template <int W> __device__ __forceinline__ uint32_t lds_peek(uint32_t U) {
  return __builtin_amdgcn_readfirstlane(*(volatile lds_u32*)(size_t)(U + (W * 4 - 4)));
}
template <int W, int SLEEP> __device__ __forceinline__ void lds_wait(uint32_t U, uint32_t n) {
  uint32_t v, c;
  if (SLEEP)
    asm volatile(
        "s_branch 2f\n1:\n\ts_sleep %5\n2:\n\tds_read_b32 %0, %2 offset:%4\n\ts_waitcnt lgkmcnt(0)\n\tv_readfirstlane_b32 %1, %0\n\t"
        "s_cmp_lt_u32 %1, %3\n\ts_cbranch_scc1 1b"
        : "=&v"(v), "=&s"(c)
        : "v"(U), "s"(n * U), "n"(W * 4 - 4), "n"(SLEEP)
        : "memory", "scc");
  else
    asm volatile(
        "1:\n\tds_read_b32 %0, %2 offset:%4\n\ts_waitcnt lgkmcnt(0)\n\tv_readfirstlane_b32 %1, %0\n\t"
        "s_cmp_lt_u32 %1, %3\n\ts_cbranch_scc1 1b"
        : "=&v"(v), "=&s"(c)
        : "v"(U), "s"(n * U), "n"(W * 4 - 4)
        : "memory", "scc");
}
__device__ __forceinline__ int lane_id() {
  int l;
  asm volatile("v_mbcnt_lo_u32_b32 %0, -1, 0\n\tv_mbcnt_hi_u32_b32 %0, -1, %0" : "=v"(l));
  return l;
}
__device__ __forceinline__ uint32_t wave_max_u32(uint32_t v) {
  asm volatile(
      "s_nop 1\n\tv_max_u32_dpp %0, %0, %0 quad_perm:[1,0,3,2] row_mask:0xf bank_mask:0xf\n\t"
      "s_nop 1\n\tv_max_u32_dpp %0, %0, %0 quad_perm:[2,3,0,1] row_mask:0xf bank_mask:0xf\n\t"
      "s_nop 1\n\tv_max_u32_dpp %0, %0, %0 row_half_mirror row_mask:0xf bank_mask:0xf\n\t"
      "s_nop 1\n\tv_max_u32_dpp %0, %0, %0 row_mirror row_mask:0xf bank_mask:0xf\n\t"
      "s_nop 1\n\tv_max_u32_dpp %0, %0, %0 row_bcast:15 row_mask:0xa bank_mask:0xf\n\t"
      "s_nop 1\n\tv_max_u32_dpp %0, %0, %0 row_bcast:31 row_mask:0xc bank_mask:0xf\n\ts_nop 1"
      : "+v"(v));
  return (uint32_t)__builtin_amdgcn_readlane((int)v, 63);
}
__device__ __forceinline__ float sel_f(uint64_t m, float a, float b) {
  float r;
  asm("v_cndmask_b32_e64 %0, %1, %2, %3" : "=v"(r) : "v"(b), "v"(a), "s"(m));
  return r;
}
__device__ __forceinline__ cf sel_c(uint64_t m, cf a, cf b) { return cf{sel_f(m, a.x, b.x), sel_f(m, a.y, b.y)}; }
__device__ __forceinline__ u64 ld_dev(const u64* p) { return __hip_atomic_load(p, __ATOMIC_RELAXED, __HIP_MEMORY_SCOPE_AGENT); }
__device__ __forceinline__ void st_dev(u64* p, u64 v) { __hip_atomic_store(p, v, __ATOMIC_RELAXED, __HIP_MEMORY_SCOPE_AGENT); }
__device__ __forceinline__ void st_dev(uint32_t* p, uint32_t v) {
  __hip_atomic_store(p, v, __ATOMIC_RELAXED, __HIP_MEMORY_SCOPE_AGENT);
}
__device__ __forceinline__ f32x4 mfma_h(u32x4 a, u32x4 b, f32x4 c) {
  return __builtin_amdgcn_mfma_f32_16x16x32_f16(__builtin_bit_cast(f16x8, a), __builtin_bit_cast(f16x8, b), c, 0, 0, 0);
}
__device__ __forceinline__ f32x4 mfma_b(u32x4 a, u32x4 b, f32x4 c) {
  return __builtin_amdgcn_mfma_f32_16x16x32_bf16(__builtin_bit_cast(bf16x8, a), __builtin_bit_cast(bf16x8, b), c, 0, 0, 0);
}
struct PQ {
  u32x4 p, q;
};
constexpr f32x4 Z4 = f32x4{0.f, 0.f, 0.f, 0.f};
constexpr unsigned SPIN_MAX = 1u << 20;

constexpr size_t WS_EPOCH = 0;
constexpr size_t WS_F1 = 4096;
constexpr size_t WS_P2 = 8192;
constexpr size_t WS_HF = 1u << 19;
constexpr size_t WS_AGG = 1u << 20;
constexpr size_t WS_ZB = 2u << 20;
constexpr size_t WS_ZC = 8u << 20;
constexpr size_t WS_HS = 18u << 20;
constexpr size_t WS_PS = 26u << 20;

constexpr int NX = 13;
constexpr int G0_STAGE = 32768;
constexpr int L_Z = 0;
constexpr int L_X = 131072;
constexpr int L_TB = L_X + 16384;
constexpr int L_WS = L_TB + 4096;
constexpr int L_PART = L_WS + 4096;
constexpr int L_GRP = L_PART + 3584;
constexpr int L_RS = L_GRP + 2048;
constexpr int L_SYNC = -256;
constexpr int L_PRM = L_RS + 1024;
constexpr int L_SCR2 = L_PRM + 1024;
constexpr int LDS_BYTES = L_SCR2 + 256 + 256;
enum { SW_PART = 1, SW_GRP, SW_PUB, SW_WMAX, SW_XMAX, SW_HD, SW_G1, SW_FS, SW_G0E, SW_HS, SW_LD, SW_RS, SW_F1, SW_PX, SW_FR, SW_PL, SW_P2 };

struct Args {
  const uint32_t* x2;
  const u16 *w_r, *w_i, *xB, *Lre, *Lim, *Bre, *Bim, *Cre, *Cim, *ldt, *Dsk;
  float* out;
  char* ws;
};

struct Tile {
  int tm, tn, nt, late;
};
__device__ __forceinline__ Tile tile_map() {
  const int bid = bid_x(), late = bid >> 7, cq = (bid & 1) * 4 + ((bid >> 5) & 3);
  return Tile{(bid >> 1) & 15, late * 8 + cq, cq, late};
}
__device__ __forceinline__ u32x4 lds4(const void* p) { return *reinterpret_cast<const u32x4*>(p); }
__device__ __forceinline__ u32x4 ldsa(uint32_t a) { return *(__attribute__((address_space(3))) u32x4*)(size_t)a; }
__device__ __forceinline__ void lds_cf2(const void* p, cf& a, cf& b) {
  const f32x4 v = *reinterpret_cast<const f32x4*>(p);
  a = cf{v[0], v[1]};
  b = cf{v[2], v[3]};
}
template <int W> __device__ __forceinline__ void group_sync(uint32_t U, uint32_t n) {
  wait_lgkm0();
  lds_signal<W>(U);
  lds_wait<W, 0>(U, n);
}
__device__ __forceinline__ int k_rot() { return (bid_x() & 6) * 2 + (bid_x() & 1) * 8; }
__device__ __forceinline__ void stage0(const Args& A, char* smem, int wave, int lane, int kt) {
  const Tile T = tile_map();
  const int prow = lane >> 3;
  const uint32_t va = (T.tm * 128 + wave * 32 + prow) * 2048 + ((lane & 7) ^ (prow & 3)) * 16;
  const uint32_t vb = (T.tn * 64 + wave * 16 + prow) * 2048 + ((lane & 7) ^ prow) * 16;
  const uint32_t dst = lds_base(smem) + wave * 4096 + (kt & 3) * G0_STAGE;
  kt = (kt + k_rot()) & 15;
  glds4(A.xB + kt * 64, A.xB + kt * 64, va, va + 15360, (va ^ 64) + 2 * 15360, (va ^ 64) + 3 * 15360, dst);
  glds4(A.Bre + kt * 64, A.Bim + kt * 64 - 1024, vb, vb + 15360, vb, vb + 15360, dst + 16384);
}
__device__ __forceinline__ int g1_kt(int late, int s) {
  return s < 8 ? ((s + 6) & 7) + (late ? 0 : 8) : ((s + (late ? 8 : 32 - NX) + 13) & 15) + 16;
}
__device__ __forceinline__ uint32_t g1_vc(int wave, int lane) {
  return (tile_map().nt * 128 + (wave >> 1) * 64 + (wave & 1) * 2 + (lane >> 3) * 4) * 2048 + ((lane & 7) ^ (lane >> 3)) * 16;
}
__device__ __forceinline__ void stage_c(const Args& A, int wave, int lane, int s) {
  const int kt = g1_kt(tile_map().late, s);
  const u16* src = ((kt & 1) ? A.Cim : A.Cre) + (kt >> 1) * 64;
  const uint32_t vc = g1_vc(wave, lane);
  glds4(src, src, vc, vc + 65536 - 1024, vc, vc + 65536 - 1024, 256 + ((s + 1) & 3) * G0_STAGE + 16384 + wave * 4096);
}
__device__ __forceinline__ void warm1(char* smem, const void* base, uint32_t off) {
  uint32_t keep;
  asm volatile("s_mov_b32 %0, m0\n\ts_mov_b32 m0, %3\n\ts_nop 0\n\tglobal_load_lds_dword %1, %2\n\ts_mov_b32 m0, %0"
               : "=&s"(keep)
               : "v"(off), "s"(base), "s"(lds_base(smem) + L_SCR2)
               : "memory");
}
__device__ __forceinline__ void warm_dev(char* smem, const void* base, uint32_t off) {
  uint32_t keep;
  asm volatile("s_mov_b32 %0, m0\n\ts_mov_b32 m0, %3\n\ts_nop 0\n\tglobal_load_lds_dword %1, %2 sc1\n\ts_mov_b32 m0, %0"
               : "=&s"(keep)
               : "v"(off), "s"(base), "s"(lds_base(smem) + L_SCR2)
               : "memory");
}
typedef short s16x2 __attribute__((ext_vector_type(2)));
__device__ __forceinline__ uint32_t pmax(uint32_t m, uint32_t u) {
  return __builtin_bit_cast(uint32_t, __builtin_elementwise_max(__builtin_bit_cast(s16x2, m), __builtin_bit_cast(s16x2, u & 0x7FFF7FFFu)));
}
__device__ __forceinline__ uint32_t pmax_wave(uint32_t m) {
  const uint32_t h = m << 16;
  return wave_max_u32(h > m ? h : m);
}
__device__ __forceinline__ void warm_r(const Args& A, char* smem, int lane, int c, int i) {
  const int bid = bid_x(), tm = ((bid >> 1) & 3) + (c & 3) * 4, nt = (bid & 1) * 4 + (c >> 2);
  uint64_t px = (uint64_t)A.xB, pbr = (uint64_t)A.Bre, pbi = (uint64_t)A.Bim;
  asm volatile("" : "+s"(px), "+s"(pbr), "+s"(pbi));
  px += (tm * 128 + (nt & 3) * 32 + (lane & 31)) * 2048;
  const uint64_t pb = ((lane & 16) ? pbi : pbr) + (nt * 64 + (c & 3) * 16 + (lane & 15)) * 2048;
  const int hi = lane >> 5;
  const bool k2 = i == 4 || i == 6;
  const int sl = i < 3 ? i + 1 : (k2 ? i - 4 : i + 1) + hi;
  const uint64_t pfp = ((k2 || (i < 3 && hi)) ? pb + (k2 ? 1 << 20 : 0) : px) + ((sl + k_rot()) & 15) * 128;
  uint32_t keep;
  asm volatile("s_mov_b32 %0, m0\n\ts_mov_b32 m0, %2\n\ts_nop 0\n\tglobal_load_lds_dword %1, off\n\ts_mov_b32 m0, %0"
               : "=&s"(keep)
               : "v"(pfp), "s"(lds_base(smem) + L_SCR2)
               : "memory");
}
__device__ __forceinline__ uint32_t pk_mul_h(uint32_t a, uint32_t b) {
  return __builtin_bit_cast(uint32_t, __builtin_bit_cast(f16x2, a) * __builtin_bit_cast(f16x2, b));
}
__device__ __forceinline__ u32x2 lds_tr(uint32_t a) {
  return __builtin_bit_cast(u32x2, __builtin_amdgcn_ds_read_tr16_b64_v4i16((__attribute__((address_space(3))) s16x4*)(size_t)a));
}
__device__ __forceinline__ void sta4(uint32_t a, u32x4 v) { *(__attribute__((address_space(3))) u32x4*)(size_t)a = v; }
template <int P> __device__ __forceinline__ void prio(bool on) {
  __builtin_amdgcn_sched_barrier(0);
  if (on) __builtin_amdgcn_s_setprio(P);
  __builtin_amdgcn_sched_barrier(0);
}
__device__ __forceinline__ void row_head(u32x4 (&x)[2][2], const PQ& m16, const PQ& m1, const cf (&tw)[2][4], bool on) {
  f32x4 dr[2][2], di[2][2];
#pragma unroll
  for (int k = 0; k < 2; ++k) {
    const f32x4 ur = mfma_h(x[k][0], m16.p, Z4), ui = mfma_h(x[k][0], m16.q, Z4);
    const u32x4 xn = x[k][1] ^ 0x80008000u;
    dr[k][0] = mfma_h(x[k][1], m1.p, ur), di[k][0] = mfma_h(x[k][1], m1.q, ui);
    dr[k][1] = mfma_h(xn, m1.p, ur), di[k][1] = mfma_h(xn, m1.q, ui);
  }
  prio<0>(on);
#pragma unroll
  for (int i = 0; i < 16; ++i) {
    const int k = i >> 3, t = (i >> 2) & 1, r = i & 3;
    const cf v = fin(cmul(cf{dr[k][t][r], di[k][t][r]}, tw[t][r]));
    dr[k][t][r] = v.x, di[k][t][r] = v.y;
  }
  prio<1>(on);
#pragma unroll
  for (int i = 0; i < 16; ++i) x[i >> 3][(i >> 2) & 1][i & 3] = pk_h(dr[i >> 3][(i >> 2) & 1][i & 3], di[i >> 3][(i >> 2) & 1][i & 3]);
}
template <int W, bool B = false> __device__ __forceinline__ void poll_count(char* ws, int idx, uint32_t n, uint32_t U) {
  uint32_t c0, c1, c2, t, lim = SPIN_MAX;
#define ASK(c) "global_atomic_add " c ", %5, %5, %6 sc0\n\t"
#define CHK(c) "s_waitcnt vmcnt(%10)\n\tv_readfirstlane_b32 %3, " c "\n\ts_cmp_ge_u32 %3, %7\n\ts_cbranch_scc1 2f\n\t" ASK(c)
  asm volatile(
      "s_mov_b64 exec, 1\n\t" ASK("%0") "s_sleep 4\n\t" ASK("%1") "s_sleep 4\n\t" ASK("%2") ".if %11\n\ts_barrier\n.endif\n1:\n\t" CHK("%0") CHK("%1") CHK("%2")
      "s_sub_u32 %4, %4, 1\n\ts_cbranch_scc0 1b\n2:\n\tds_add_u32 %8, %8 offset:%9\n\ts_mov_b64 exec, -1\n\ts_waitcnt vmcnt(0)"
      : "=&v"(c0), "=&v"(c1), "=&v"(c2), "=&s"(t), "+s"(lim)
      : "v"(0), "s"(ws + WS_HF + idx * 128), "s"(n), "v"(U), "n"(W * 4 - 4), "n"(2), "n"(B)
      : "memory", "scc");
#undef ASK
#undef CHK
}
__device__ __forceinline__ bool fft_role(const Args& A, char* smem, int f, int lane) {
  char* const ws = A.ws;
  const Tile T = tile_map();
  const int bid = bid_x(), li = bid & 127;
  int l15 = lane & 15, q = lane >> 4;
  lds_u32* const sw = (lds_u32*)(smem + L_SYNC);
  const uint32_t U = (uint32_t)(size_t)(sw + 1), ldsb = lds_base(smem);
  const __amdgpu_buffer_rsrc_t rzb = __builtin_amdgcn_make_buffer_rsrc((void*)(ws + WS_ZB), 0, 16 << 20, 0x00020000);
  auto fsync = [&](int n) { group_sync<SW_FS>(U, 8 * n); };
  auto bars = [&](int n) {
#pragma nounroll
    for (int i = 0; i < n; ++i) bar();
  };
  u64 epoch = 0;
  if (f == 0) epoch = ld_dev((const u64*)(ws + WS_EPOCH));
  if (f == 7) {
    const __amdgpu_buffer_rsrc_t ra = __builtin_amdgcn_make_buffer_rsrc((void*)(ws + WS_AGG), 0, 512 << 10, 0x00020000);
    __builtin_amdgcn_raw_buffer_store_b128(u32x4{0u, 0u, 0u, 0u}, ra, (T.tm * 2048 + T.tn * 64 + lane) * 16, 0, 16);
    __builtin_amdgcn_raw_buffer_store_b128(u32x4{0u, 0u, 0u, 0u}, ra, (T.tm * 2048 + 1024 + T.tn * 64 + lane) * 16, 0, 16);
    if (!T.late && lane < (T.tn ? 1 : 4)) st_dev((uint32_t*)(ws + WS_HF + (lane ? 4 * T.tm + lane - 1 : 64 + li) * 128), 0u);
    if (lane == 0) st_dev((uint32_t*)(ws + WS_P2) + bid, 0u);
  }
  __builtin_amdgcn_sched_barrier(0);
  auto ks_of = [&](int slot) {
    const int w = slot & 7, k = bid ? bid + 256 * w : (w < 4 ? 256 * w : 256 * w - 896);
    return slot < 8 ? k : (k ? 2048 - k : 1024);
  };
  {
    const int e = f & 3, n = 4 * q + e;
    uint32_t* tb = reinterpret_cast<uint32_t*>(smem + L_TB) + lane * 4;
    uint32_t* tx = reinterpret_cast<uint32_t*>(smem + L_X) + lane * 4;
    cf c = cis<256>(n * l15 * (f < 4 ? 16 : 1));
    if (f < 4) {
      c = c * 0.25f;
      tb[e] = pk_h(c.x, -c.y);
      tb[256 + e] = pk_h(c.y, c.x);
      if (T.late) {
        const uint32_t ph = pk_bf(c.x, -c.y), qh = pk_bf(c.y, c.x);
        const float lr = c.x - u2f(ph << 16), lo = c.y - u2f(qh << 16);
        tx[e] = ph;
        tx[256 + e] = qh;
        tx[512 + e] = pk_bf(lr, -lo);
        tx[768 + e] = pk_bf(lo, lr);
      } else {
        const cf d = cis<32>((2 * n + 1) * l15) * 0.25f;
        tx[e] = pk_h(d.x, -d.y);
        tx[256 + e] = pk_h(d.y, d.x);
      }
    } else {
      *reinterpret_cast<cf*>(tb + 512 + (e >> 1) * 256 + (e & 1) * 2) = c;
    }
    if (!T.late) {
      *reinterpret_cast<cf*>(tx + 512 + (f >> 1) * 256 + (f & 1) * 2) = cis<512>(n * (16 * (f >> 2) + l15)) * 0.5f;
      const cf w = cis<1024>(l15 + 16 * (f >> 2) + 32 * n);
      *reinterpret_cast<cf*>(tx + 1536 + (f >> 1) * 256 + (f & 1) * 2) = cf{w.y, -w.x};
      const int slot = f < 4 ? l15 : n, u = f < 4 ? q >> 1 : l15 & 1, j = f < 4 ? n & 7 : l15 >> 1;
      const cf g = cis<2048>(j * ks_of(slot)) * (((bid ? slot >> 3 : (slot >> 2) & 1) == u) ? 0.5f : 0.f);
      const uint32_t cj = slot < 8 ? 0u : 0x80000000u;
      tx[2560 + (f >> 2) * 512 + e] = pk_h(g.x, -g.y) ^ (f < 4 ? 0u : cj);
      tx[2816 + (f >> 2) * 512 + e] = pk_h(g.y, g.x) ^ (f < 4 ? cj | cj >> 16 : cj);
    }
  }
  if (!T.late) {
    if (f == 0) wait_vm<1>(); else if (f == 7) wait_vm<4>(); else wait_vm<0>();
  }
  bar_lds();
  PQ m16, m1, m8;
  cf tw[2][4];
  u32x4 wv[4];
  uint32_t pay = 0;
  m16.p = lds4(smem + L_TB + lane * 16), m16.q = lds4(smem + L_TB + 1024 + lane * 16);
  if (T.late) {
    const char* tab = smem + L_X + lane * 16;
    const uint32_t xl = ldsb + 3 * G0_STAGE + f * 4096 + lane * 16;
    const uint32_t xc = ldsb + 3 * G0_STAGE + (l15 + 64 * q) * 128 + (f ^ (l15 >> 1)) * 16;
    PQ mh, ml;
    mh.p = lds4(tab), mh.q = lds4(tab + 1024), ml.p = lds4(tab + 2048), ml.q = lds4(tab + 3072);
    lds_cf2(smem + L_TB + 2048 + lane * 16, tw[0][0], tw[0][1]), lds_cf2(smem + L_TB + 3072 + lane * 16, tw[0][2], tw[0][3]);
    if (f == 0) wait_vm<1>(); else if (f == 7) wait_vm<3>(); else wait_vm<0>();
    {
      uint32_t mx = 0;
#pragma unroll
      for (int i = 0; i < 4; ++i) wv[i] = ldsa(xl + i * 1024);
#pragma unroll
      for (int i = 0; i < 16; ++i) mx = pmax(mx, wv[i >> 2][i & 3]);
      const uint32_t ew = pmax_wave(mx);
      if (lane == 0) __hip_atomic_fetch_max(sw + SW_XMAX, ew, __ATOMIC_RELAXED, __HIP_MEMORY_SCOPE_WORKGROUP);
    }
    fsync(1);
#pragma unroll
    for (int e = 0; e < 4; ++e) wv[e] = ldsa(xc + e * 2048);
    int ecu = (int)(lds_peek<SW_XMAX>(U) >> 23);
    ecu = ecu < 26 ? 26 : ecu;
    pay = (uint32_t)ecu;
    float sc = u2f((uint32_t)(257 - ecu) << 23);
    asm volatile("" : "+v"(sc));
#pragma unroll
    for (int r = 0; r < 4; ++r) tw[0][r] = tw[0][r] * sc;
    f32x4 dr[4], di[4];
#pragma unroll
    for (int c = 0; c < 4; ++c) {
      const u32x4 x = u32x4{wv[0][c], wv[1][c], wv[2][c], wv[3][c]};
      dr[c] = mfma_b(x, mh.p, mfma_b(x, ml.p, Z4));
      di[c] = mfma_b(x, mh.q, mfma_b(x, ml.q, Z4));
    }
#pragma unroll
    for (int i = 0; i < 16; ++i) {
      const cf v = fin(cmul(cf{dr[i >> 2][i & 3], di[i >> 2][i & 3]}, tw[0][i & 3]));
      dr[i >> 2][i & 3] = v.x, di[i >> 2][i & 3] = v.y;
    }
#pragma unroll
    for (int c = 0; c < 4; ++c) {
      const u32x4 a = u32x4{pk_h(dr[c][0], di[c][0]), pk_h(dr[c][1], di[c][1]), pk_h(dr[c][2], di[c][2]), pk_h(dr[c][3], di[c][3])};
      dr[c] = mfma_h(m16.p, a, Z4);
      di[c] = mfma_h(m16.q, a, Z4);
    }
#pragma unroll
    for (int r = 0; r < 4; ++r)
      sta4(xc + r * 2048, u32x4{pk_h(dr[0][r], di[0][r]), pk_h(dr[1][r], di[1][r]), pk_h(dr[2][r], di[2][r]), pk_h(dr[3][r], di[3][r])});
    fsync(2);
#pragma unroll
    for (int i = 0; i < 4; ++i) wv[i] = ldsa(xl + i * 1024);
    const int zo = (32 * f + (lane >> 3)) * 16384 + (li & 7) * 2048 + (li >> 3) * 128 + ((lane & 7) ^ q) * 16;
    asm volatile("" : "+v"(wv[0]), "+v"(wv[1]), "+v"(wv[2]), "+v"(wv[3]));
#pragma unroll
    for (int i = 0; i < 4; ++i) __builtin_amdgcn_raw_buffer_store_b128(wv[i], rzb, (zo ^ ((i & 1) * 64)) + i * 131072, 0, 16);
    bars(2);
  } else {
    const char* tab = smem + L_X + lane * 16;
    m1.p = lds4(tab), m1.q = lds4(tab + 1024), m8.p = lds4(tab + 10240), m8.q = lds4(tab + 11264);
#pragma unroll
    for (int i = 0; i < 4; ++i) lds_cf2(tab + 2048 + i * 1024, tw[i >> 1][(i & 1) * 2], tw[i >> 1][(i & 1) * 2 + 1]);
  }
  wait_vm<0>();
  asm volatile("" : "+v"(epoch));
  const u64 tag = ((epoch + 1) & 0x7FFFFFFFFFFFull) | 0x800000000000ull;
  u64 f1 = (tag << 16) | (u64)pay;
  asm volatile("" : "+v"(f1));
  bar();
  if (f == 0 && lane == 0) st_dev((u64*)(ws + WS_F1) + bid, f1);
  int nb = T.late ? 10 : 11;
  if (T.late && f >= 6) {
    uint64_t pbr = (uint64_t)A.Bre, pbi = (uint64_t)A.Bim;
    asm volatile("" : "+s"(pbr), "+s"(pbi));
    uint64_t pf = ((lane & 16) ? pbi : pbr) + (T.tn * 64 + (T.tm >> 2) * 16 + l15) * 2048 + ((2 * f + (lane >> 5) + k_rot()) & 15) * 128;
    asm volatile("" : "+v"(pf));
    bars(f - 4);
    nb -= f - 4;
    __builtin_amdgcn_s_sleep(5);
    uint32_t keep;
    asm volatile("s_mov_b32 %0, m0\n\ts_mov_b32 m0, %2\n\ts_nop 0\n\tglobal_load_lds_dword %1, off\n\ts_mov_b32 m0, %0"
                 : "=&s"(keep)
                 : "v"(pf), "s"(ldsb + L_SCR2)
                 : "memory");
  }
  bars(nb);
  int zo = (q < 2 ? bid : (bid ? 256 - bid : 128)) * 16384 + (q & 1) * 8192 + f * 256 + l15 * 16;
  asm volatile("" : "+v"(zo));
  uint32_t ex = 0, e2 = 0, e3 = 0;
  if (f == 0) {
    u64 v[4];
    auto poll1 = [&]() {
#pragma unroll
      for (int i = 0; i < 4; ++i) v[i] = ld_dev((const u64*)(ws + WS_F1) + lane + 64 * i);
    };
    __builtin_amdgcn_s_sleep(5);
    poll1();
    bar();
    __builtin_amdgcn_s_setprio(2);
#pragma nounroll
    for (unsigned spins = 0; spins < SPIN_MAX; ++spins) {
      bool ok = true;
#pragma unroll
      for (int i = 0; i < 4; ++i) ok &= (v[i] >> 16) == tag;
      if (__builtin_amdgcn_ballot_w64(!ok) == 0) break;
      __builtin_amdgcn_s_sleep(16);
      poll1();
    }
    lds_signal<SW_F1>(U);
    e2 = (uint32_t)v[2] & 255u, e3 = (uint32_t)v[3] & 255u;
    asm volatile("" : "+v"(e2), "+v"(e3));
  } else {
    if (T.late && f >= 4) {
      __builtin_amdgcn_s_sleep(6);
      warm1(smem, (f & 1) ? A.Cre : A.Cim, (T.nt * 32 + T.tm * 2 + ((f >> 1) & 1)) * 8192 + lane * 128);
    }
    bar();
  }
  u32x4 zi[4];
  if (!T.late) {
    if (f) lds_wait<SW_F1, 1>(U, 1);
#pragma unroll
    for (int e = 0; e < 4; ++e) zi[e] = __builtin_amdgcn_raw_buffer_load_b128(rzb, zo + e * 2048, 0, 16);
  }
  if (f == 0) {
    ex = wave_max_u32(e2 > e3 ? e2 : e3);
    const float s2 = __builtin_amdgcn_ldexpf(1.0f, (int)e2 - (int)ex), s3 = __builtin_amdgcn_ldexpf(1.0f, (int)e3 - (int)ex);
    uint32_t* rs = reinterpret_cast<uint32_t*>(smem + L_RS) + lane;
    rs[0] = pk_h(s2, s2);
    rs[64] = pk_h(s3, s3);
    wait_lgkm0();
    lds_signal<SW_RS>(U);
    __builtin_amdgcn_s_setprio(0);
  }
  if (f == 0 && lane == 0) {
    if (T.late) st_dev((uint32_t*)(ws + WS_P2) + bid, 256u);
    if (bid == 0) st_dev((u64*)(ws + WS_EPOCH), epoch + 1);
  }
  if ((bid | f) == 0) __builtin_amdgcn_s_setprio(3);
  if (!T.late) {
    const __amdgpu_buffer_rsrc_t rr = __builtin_amdgcn_make_buffer_rsrc((void*)A.w_r, 0, 2048 * 1026, 0x00020000);
    const __amdgpu_buffer_rsrc_t ri = __builtin_amdgcn_make_buffer_rsrc((void*)A.w_i, 0, 2048 * 1026, 0x00020000);
    uint32_t sr, si;
    auto wload = [&](int ka) {
      const int oa = ka * 1026, ob = ((2048 - ka) & 2047) * 1026;
      wv[0] = __builtin_amdgcn_raw_buffer_load_b128(rr, lane * 16, oa & ~3, 2);
      wv[1] = __builtin_amdgcn_raw_buffer_load_b128(ri, lane * 16, oa & ~3, 2);
      wv[2] = __builtin_amdgcn_raw_buffer_load_b128(rr, lane * 16, (ob + 2) & ~3, 2);
      wv[3] = __builtin_amdgcn_raw_buffer_load_b128(ri, lane * 16, (ob + 2) & ~3, 2);
      const int so = lane == 0 ? ob : (lane == 1 ? oa + 1024 : ((ka & 1) ? oa + 1022 : ob + 1024));
      sr = __builtin_amdgcn_raw_buffer_load_b16(rr, so, 0, 2);
      si = __builtin_amdgcn_raw_buffer_load_b16(ri, so, 0, 2);
    };
    const bool two = (bid | f) == 0;
    u32x4 w2r, w2i;
    uint32_t s2r, s2i;
    if (two) {
      wload(1024);
      w2r = wv[0], w2i = wv[1], s2r = sr, s2i = si;
    }
    wload(ks_of(f));
    __builtin_amdgcn_sched_barrier(0);
    lds_wait<SW_RS, 1>(U, 1);
    {
      const u32x4 rs = lds4(smem + L_RS + f * 64 + (l15 >> 3) * 32 + (q & 1) * 16);
      u32x4 o[4];
      asm volatile("" : "+v"(zi[0]), "+v"(zi[1]), "+v"(zi[2]), "+v"(zi[3]));
#pragma unroll
      for (int c = 0; c < 4; ++c) {
        u32x4 a;
#pragma unroll
        for (int e = 0; e < 4; ++e) a[e] = pk_mul_h(zi[e][c], rs[e]);
        const f32x4 re = mfma_h(a, m8.p, Z4), im = mfma_h(a, m8.q, Z4);
#pragma unroll
        for (int r = 0; r < 4; ++r) o[r][c] = pk_h(re[r], im[r]);
      }
      const uint32_t oa = l15 * 2048 + f * 256 + ((4 * q) ^ (l15 & 7) ^ ((f & 2) << 1)) * 16;
#pragma unroll
      for (int r = 0; r < 4; ++r) sta4(ldsb + L_Z + (oa ^ (r * 16)), o[r]);
    }
    __builtin_amdgcn_sched_barrier(0);
    uint32_t wsp;
    {
      auto shift = [](u32x4& w, uint32_t last) {
        const uint32_t nx = (uint32_t)__builtin_amdgcn_update_dpp((int)last, (int)(uint32_t)w[0], 0x130, 0xF, 0xF, false);
        w = u32x4{__builtin_amdgcn_alignbit(w[1], w[0], 16), __builtin_amdgcn_alignbit(w[2], w[1], 16),
                  __builtin_amdgcn_alignbit(w[3], w[2], 16), __builtin_amdgcn_alignbit(nx, w[3], 16)};
      };
      if (bid & 1) shift(wv[0], sr), shift(wv[1], si);
      else shift(wv[2], sr), shift(wv[3], si);
      wsp = sr | si << 16;
      uint32_t mx = 0;
      if (two) {
        sta4(ldsb + L_WS + lane * 16, w2r), sta4(ldsb + L_WS + 1024 + lane * 16, w2i);
#pragma unroll
        for (int i = 0; i < 4; ++i) mx = pmax(pmax(mx, w2r[i]), w2i[i]);
        shift(w2r, s2r), shift(w2i, s2i);
        sta4(ldsb + L_WS + 2048 + lane * 16, w2r), sta4(ldsb + L_WS + 3072 + lane * 16, w2i);
        wsp = (uint32_t)__builtin_amdgcn_update_dpp((int)wsp, (int)(s2r | s2i << 16), 0x114, 0xF, 0x2, false);
      }
      mx = pmax(mx, wsp);
#pragma unroll
      for (int i = 0; i < 16; ++i) mx = pmax(mx, wv[i >> 2][i & 3]);
      const uint32_t wmx = pmax_wave(mx);
      if (lane == 0) __hip_atomic_fetch_max(sw + SW_WMAX, wmx, __ATOMIC_RELAXED, __HIP_MEMORY_SCOPE_WORKGROUP);
    }
    fsync(1);
    pay = lds_peek<SW_WMAX>(U) >> 23;
    pay = pay < 26u ? 26u : pay;
    float fs = __builtin_amdgcn_ldexpf(0.25f, 125 - (int)pay);
    asm volatile("" : "+v"(fs));
    prio<1>(!two);
    const uint32_t row = ldsb + L_Z + f * 2048;
    u32x4 x[2][2];
    auto rows = [&]() {
      const uint32_t ra = row + q * 512 + ((l15 >> 2) ^ f ^ ((q & 1) << 2)) * 16 + (l15 & 3) * 4;
#pragma unroll
      for (int i = 0; i < 16; ++i) x[i >> 3][(i >> 2) & 1][i & 3] = *(lds_u32*)(size_t)((ra ^ ((i & 4) * 16)) + (i & 3) * 128 + (i >> 3) * 16384);
    };
    rows();
    if (two) x[1][0] = x[0][0] ^ 0x80000000u, x[1][1] = x[0][1] ^ 0x80000000u;
    uint32_t wa = row, wb = two ? ldsb + L_X + 14336 : row + 16384;
    asm volatile("" : "+v"(x[0][0]), "+v"(x[0][1]), "+v"(x[1][0]), "+v"(x[1][1])::"memory");
    sta4(wa + lane * 16, wv[0]);
    sta4(wa + 1024 + lane * 16, wv[1]);
    sta4(wb + lane * 16, wv[2]);
    sta4(wb + 1024 + lane * 16, wv[3]);
#pragma nounroll
    for (int jb = 0;; jb = 4) {
      u32x2 fw[4][2];
#pragma unroll
      for (int h = 0; h < 2; ++h) {
        fw[0][h] = lds_tr(wa + q * 256 + l15 * 8 + h * 128);
        fw[1][h] = lds_tr(wa + q * 256 + l15 * 8 + h * 128 + 1024);
        fw[2][h] = lds_tr(wb + (3 - q) * 256 + l15 * 8 + h * 128);
        fw[3][h] = lds_tr(wb + (3 - q) * 256 + l15 * 8 + h * 128 + 1024);
      }
      row_head(x, m16, m1, tw, !two);
#pragma unroll
      for (int i = 0; i < 8; ++i)
        fw[2 + (i >> 2)][(i >> 1) & 1][i & 1] =
            (uint32_t)__builtin_amdgcn_update_dpp(0, (int)(uint32_t)fw[2 + (i >> 2)][(i >> 1) & 1][i & 1], 0x140, 0xF, 0xF, true);
      const uint32_t s0 = __builtin_amdgcn_readlane(wsp, jb), s1 = __builtin_amdgcn_readlane(wsp, jb + 1);
#pragma unroll
      for (int t = 0; t < 2 - 0; ++t) {
        cf u[4];
        lds_cf2(smem + L_X + 6144 + t * 2048 + lane * 16, u[0], u[1]), lds_cf2(smem + L_X + 7168 + t * 2048 + lane * 16, u[2], u[3]);
        f32x4 ar = mfma_h(m16.p, x[0][t], Z4), ai = mfma_h(m16.q, x[0][t], Z4), br = mfma_h(m16.p, x[1][t], Z4), bi = mfma_h(m16.q, x[1][t], Z4);
        auto half = [](uint32_t w, int hi) { return u2f(hi ? w & 0xFFFF0000u : w << 16); };
        cf lo[4];
#pragma unroll
        for (int r = 0; r < 4; ++r) {
          const int rr = t ? 3 - r : r;
          lo[r] = fin(cf{half(fw[2 * t][rr >> 1][rr & 1], 0), half(fw[2 * t + 1][rr >> 1][rr & 1], 0)});
        }
        prio<0>(!two);
#pragma unroll
        for (int r = 0; r < 4; ++r) {
          const int rr = t ? r : 3 - r;
          const cf hi = cf{half(fw[2 - 2 * t][rr >> 1][rr & 1], 1), half(fw[3 - 2 * t][rr >> 1][rr & 1], 1)};
          cf fa = t ? hi : lo[r], fb = t ? lo[r] : hi;
          fb.y = -fb.y;
          if (t == 0 && r == 0 && lane == 0) {
            fa = (fa + cf{half(s0, 0), -half(s0, 1)}) * 0.5f;
            fb = (fb + cf{half(s1, 0), half(s1, 1)}) * 0.5f;
          }
          const cf tb = cf{br[r] * fs, bi[r] * fs};
          const cf E = cf{__builtin_fmaf(ar[r], fs, tb.x), __builtin_fmaf(ai[r], fs, tb.y)};
          const cf D = cf{__builtin_fmaf(ar[r], fs, -tb.x), __builtin_fmaf(ai[r], fs, -tb.y)};
          const cf X1 = cmad(u[r], D, E), X2 = cf{__builtin_fmaf(2.f, E.x, -X1.x), __builtin_fmaf(2.f, E.y, -X1.y)};
          const cf Yb = cmul(X2, fb), S = cmad(X1, fa, Yb);
          const cf Df = cf{__builtin_fmaf(-2.f, Yb.x, S.x), __builtin_fmaf(-2.f, Yb.y, S.y)};
          const cf O1 = fin(cmad(cf{u[r].x, -u[r].y}, Df, S));
          const cf O2 = fin(cf{__builtin_fmaf(2.f, S.x, -O1.x), __builtin_fmaf(-2.f, S.y, O1.y)});
          ar[r] = O1.x, ai[r] = O1.y, br[r] = O2.x, bi[r] = O2.y;
        }
        prio<1>(!two);
#pragma unroll
        for (int r = 0; r < 4; ++r) x[0][t][r] = pk_h(ar[r], -ai[r]), x[1][t][r] = pk_h(br[r], bi[r]);
      }
      if (!two) break;
      if (jb) {
        x[0][0] = ldsa(row + lane * 16), x[0][1] = ldsa(row + 1024 + lane * 16);
        break;
      }
      sta4(row + lane * 16, x[0][0]), sta4(row + 1024 + lane * 16, x[0][1]);
      rows();
      x[0][0] = x[1][0] ^ 0x80000000u, x[0][1] = x[1][1] ^ 0x80000000u;
      wa = ldsb + L_WS, wb = ldsb + L_WS + 2048;
    }
    row_head(x, m16, m1, tw, !two);
    const uint32_t oa = row + l15 * 128 + (q ^ (l15 & 7)) * 16;
#pragma unroll
    for (int i = 0; i < 4; ++i) {
      const f32x4 re = mfma_h(x[i >> 1][i & 1], m16.p, Z4), im = mfma_h(x[i >> 1][i & 1], m16.q, Z4);
      sta4((oa ^ ((i & 1) * 64)) + (i >> 1) * 16384, u32x4{pk_h(re[0], im[0]), pk_h(re[1], im[1]), pk_h(re[2], im[2]), pk_h(re[3], im[3])});
    }
    m8.p = lds4(smem + L_X + 12288 + lane * 16), m8.q = lds4(smem + L_X + 13312 + lane * 16);
    fsync(2);
    u32x4 yi[4];
#pragma unroll
    for (int e = 0; e < 4; ++e) yi[e] = lds4(smem + L_Z + (4 * q + e) * 2048 + f * 256 + (l15 ^ ((2 * f + (l15 >> 3)) & 7)) * 16);
    asm volatile("" : "+v"(yi[0]), "+v"(yi[1]), "+v"(yi[2]), "+v"(yi[3]));
    u32x4 o[4];
#pragma unroll
    for (int c = 0; c < 4; ++c) {
      const u32x4 y = u32x4{yi[0][c], yi[1][c], yi[2][c], yi[3][c]};
      const f32x4 re = mfma_h(m8.p, y, Z4), im = mfma_h(m8.q, y, Z4);
#pragma unroll
      for (int r = 0; r < 4; ++r) o[r][c] = pk_h(re[r], im[r]);
    }
    const int zc = (int)(WS_ZC - WS_ZB) + q * 4096 + f * 256 + l15 * 16;
    asm volatile("" : "+v"(o[0]), "+v"(o[1]), "+v"(o[2]), "+v"(o[3]));
    lds_wait<SW_HD, 1>(U, 4);
#pragma unroll
    for (int i = 0; i < 4; ++i)
      __builtin_amdgcn_raw_buffer_store_b128(o[i], rzb, zc + ((i & 1) ? (bid ? 256 - bid : 128) : bid) * 16384 + (i >> 1) * 2048, 0, 16);
    __builtin_amdgcn_s_setprio(0);
  } else {
    if (f >= 4) return true;
    if (0 == 6) {
      __builtin_amdgcn_s_sleep(2);
      warm1(smem, (f & 1) ? A.Cim : A.Cre, (T.nt * 128 + (T.tm >> 2) * 32 + (lane & 31)) * 2048 + ((lane >> 5) * 8 + 7 * (f >> 1)) * 128);
    }
    if (0 && 0 < 4 && f >= 2) {
      if (0 == 1) lds_wait<SW_G0E, 2>(U, 4);
      if (0 == 2) lds_wait<SW_PUB, 2>(U, 4);
      if (0 == 3) lds_wait<SW_GRP, 2>(U, 4);
      warm1(smem, (f & 1) ? A.Cim : A.Cre, (T.nt * 128 + (T.tm >> 2) * 32 + (lane & 31)) * 2048 + ((lane >> 5) * 8 + 3) * 128);
    }
    if (f == 1) {
      lds_wait<SW_PUB, 4>(U, 4);
      lds_wait<SW_F1, 4>(U, 1);
      __builtin_amdgcn_s_sleep(10);
      poll_count<SW_G1>(ws, 4 * T.tm, 16, U);
    }
  }
  const int N = T.late ? 32 - NX : NX;
  if (T.late && f != 1) return false;
  bars(2);
  wait_vm<0>();
  bar();
  lane = lane_id();
  uint32_t vo = (32 * f + (lane >> 3)) * 16384 + (li & 7) * 2048 + (li >> 3) * 128 + ((lane & 7) ^ (lane >> 4)) * 16;
  uint32_t vp = (vo ^ 64) + 131072 - 1024;
  asm volatile("" : "+v"(vo), "+v"(vp));
  u32x4 pw;
  auto scales = [&]() {
    uint32_t mw = pw[0] > pw[1] ? pw[0] : pw[1], m2 = pw[2] > pw[3] ? pw[2] : pw[3];
    mw = wave_max_u32(mw > m2 ? mw : m2);
#pragma unroll
    for (int i = 0; i < 4; ++i) {
      const float sc = __builtin_amdgcn_ldexpf(1.0f, (int)(uint32_t)pw[i] - (int)mw);
      pw[i] = pk_h(sc, sc);
    }
    if (lane == 32) pw[0] = ex + mw;
    if (lane < 33) sta4(ldsb + L_RS + lane * 16, pw);
    wait_lgkm0();
    lds_signal<SW_RS>(U);
  };
  if (f == 1) {
    bar();
    if (0) bar(); else
    {
      uint32_t c;
      asm volatile("s_mov_b64 exec, 1\n\tglobal_atomic_add %0, %1, %1, %2 sc0\n\ts_mov_b64 exec, -1\n\ts_barrier\n\ts_waitcnt vmcnt(0)"
                   : "=&v"(c)
                   : "v"(0), "s"(ws + WS_HF + (4 * T.tm + 2) * 128)
                   : "memory");
      if (__builtin_amdgcn_readfirstlane(c) < 32u) poll_count<SW_FR>(ws, 4 * T.tm + 2, 32u, U);
    }
    if (T.late) {
      bars(N - 8);
      poll_count<SW_PX, true>(ws, 64 + li, 4, U);
      return false;
    }
    bars(N - 7);
  } else if (f == 0) {
    if (lane == 0) st_dev((uint32_t*)(ws + WS_P2) + bid, 256u | pay);
    bars(N - 7);
    const __amdgpu_buffer_rsrc_t rp = __builtin_amdgcn_make_buffer_rsrc((void*)(ws + WS_P2), 0, 1024, 0x00020000);
    pw = __builtin_amdgcn_raw_buffer_load_b128(rp, lane * 16, 0, 16);
    bars(2);
#pragma nounroll
    for (unsigned spins = 0; spins < SPIN_MAX; ++spins) {
      if (__builtin_amdgcn_ballot_w64((pw[0] & pw[1] & pw[2] & pw[3] & 256u) == 0) == 0) break;
      __builtin_amdgcn_s_sleep(8);
      pw = __builtin_amdgcn_raw_buffer_load_b128(rp, lane * 16, 0, 16);
      asm volatile("" ::: "memory");
    }
    lds_signal<SW_P2>(U);
    asm volatile("" : "+v"(pw));
  } else {
    bars(N - 5);
  }
  {
    constexpr uint32_t img = 256 + ((NX - 3) & 3) * G0_STAGE;
    if (f) lds_wait<SW_P2, 1>(U, 1);
    gdev2(ws + WS_ZC, vo, vp, img + f * 4096);
    gdev2(ws + WS_ZC + 262144, vo, vp, img + f * 4096 + 2048);
    if (f == 0) scales();
    lane = lane_id();
    l15 = lane & 15;
    q = lane >> 4;
    uint32_t xc = img + (l15 + 64 * q) * 128 + (f ^ (l15 >> 1)) * 16;
    uint32_t xr = img + f * 4096 + q * 128 + ((l15 >> 1) ^ (q >> 1)) * 16 + (l15 & 1) * 8;
    int oo = (32 * f + q) * 65536 + (li & 7) * 8192 + (li >> 3) * 256 + l15 * 16;
    lds_cf2(smem + L_TB + 2048 + lane * 16, tw[0][0], tw[0][1]), lds_cf2(smem + L_TB + 3072 + lane * 16, tw[0][2], tw[0][3]);
    asm volatile("" : "+v"(xc), "+v"(xr), "+v"(oo), "+v"(tw[0][0]), "+v"(tw[0][1]), "+v"(tw[0][2]), "+v"(tw[0][3]));
    lds_wait<SW_RS, 1>(U, 2);
    u32x4 wv[4], rs;
#pragma unroll
    for (int e = 0; e < 4; ++e) {
      const int k = l15 + 64 * q + 16 * e;
      rs[e] = *(lds_u32*)(size_t)(ldsb + L_RS + ((q < 2 ? k : -k) & 127) * 4);
    }
    const int sh = __builtin_amdgcn_readfirstlane((int)*(lds_u32*)(size_t)(ldsb + L_RS + 512)) - 511;
    float sc = __builtin_amdgcn_ldexpf(1.0f, sh);
    asm volatile("" : "+v"(rs), "+v"(sc));
    wait_vm<0>();
    fsync(3);
#pragma unroll
    for (int e = 0; e < 4; ++e) wv[e] = ldsa(xc + e * 2048);
    f32x4 dr[4], di[4];
#pragma unroll
    for (int c = 0; c < 4; ++c) {
      const u32x4 x = u32x4{pk_mul_h(wv[0][c], rs[0]), pk_mul_h(wv[1][c], rs[1]), pk_mul_h(wv[2][c], rs[2]), pk_mul_h(wv[3][c], rs[3])};
      dr[c] = mfma_h(x, m16.p, Z4);
      di[c] = mfma_h(x, m16.q, Z4);
    }
#pragma unroll
    for (int c = 0; c < 4; ++c) {
      u32x4 a;
#pragma unroll
      for (int r = 0; r < 4; ++r) {
        const cf v = fin(cmul(cf{dr[c][r], di[c][r]}, tw[0][r]));
        a[r] = pk_h(v.x, v.y);
      }
      dr[c] = mfma_h(m16.p, a, Z4);
      di[c] = mfma_h(m16.q, a, Z4);
    }
    auto put = [&](auto scale) {
#pragma unroll
      for (int r = 0; r < 4; ++r) {
        u32x4 y;
#pragma unroll
        for (int c = 0; c < 4; ++c) y[c] = pk_bf(scale(dr[c][r]), scale(-di[c][r]));
        sta4(xc + r * 2048, y);
      }
    };
    if (__builtin_expect(sh >= -126 && sh <= 127, 1)) put([&](float x) { return x * sc; });
    else put([&](float x) { return __builtin_amdgcn_ldexpf(x, sh); });
    fsync(4);
    typedef __attribute__((address_space(3))) u32x2 lds_u32x2;
    u32x2 z[8];
#pragma unroll
    for (int i = 0; i < 8; ++i) z[i] = *(lds_u32x2*)(size_t)((xr ^ ((i & 3) * 32)) + i * 512);
    const __amdgpu_buffer_rsrc_t ro = __builtin_amdgcn_make_buffer_rsrc((void*)A.out, 0, 16 << 20, 0x00020000);
    lds_wait<SW_PL, 2>(U, 4);
#pragma unroll
    for (int i = 0; i < 8; ++i) {
      const uint32_t p0 = z[i][0], p1 = z[i][1];
      __builtin_amdgcn_raw_buffer_store_b128(u32x4{p0 << 16, p0 & 0xFFFF0000u, p1 << 16, p1 & 0xFFFF0000u}, ro, oo + i * 262144, 0, 2);
    }
    if (f == 0 && lane_id() < 2) st_dev((u64*)(ws + WS_F1) + bid + lane_id() * 128, 0ull);
  }
  return false;
}

__device__ __forceinline__ void publish(char* ws, int idx, int lane) {
  wait_vm<0>();
  if (lane == 0) __hip_atomic_fetch_add((uint32_t*)(ws + WS_HF + idx * 128), 1u, __ATOMIC_RELAXED, __HIP_MEMORY_SCOPE_AGENT);
}
__device__ __forceinline__ void loader_role(const Args& A, char* smem, int wave, int lane) {
  char* const ws = A.ws;
  const Tile T = tile_map();
  const int tm = T.tm, tn = T.tn;
  lds_u32* const sw = (lds_u32*)(smem + L_SYNC);
  const uint32_t U = (uint32_t)(size_t)(sw + 1);
  const uint32_t ldsb = lds_base(smem), lds0 = ldsb + wave * 4096;
  const int prow = lane >> 3;
  {
    __builtin_amdgcn_s_setprio(0);
    const uint32_t va = (tm * 128 + wave * 32 + prow) * 2048 + ((lane & 7) ^ (prow & 3)) * 16;
    const uint32_t vb = (tn * 64 + wave * 16 + prow) * 2048 + ((lane & 7) ^ prow) * 16;
    if (T.late) {
      bar();
      const int wc = (bid_x() >> 3) & 15;
      if ((wc & 7) != 3) {
#pragma nounroll
        for (int i = 2 + wave; i < 7; i += 4) warm_r(A, smem, lane, wc, i);
      }
      stage0(A, smem, wave, lane, 0);
    }
    stage0(A, smem, wave, lane, 1);
    stage0(A, smem, wave, lane, 2);
#pragma nounroll
    for (int k = 3; k < 16; ++k) {
      const uint32_t dst = lds0 + (k & 3) * G0_STAGE;
      const int ko = ((k + k_rot()) & 15) * 64;
      asm volatile(
          "s_mov_b32 m0, %9\n\ts_waitcnt vmcnt(8)\n\ts_barrier\n\t"
          "global_load_lds_dwordx4 %0, %6\n\tglobal_load_lds_dwordx4 %1, %6 offset:1024\n\t"
          "global_load_lds_dwordx4 %2, %6 offset:2048\n\tglobal_load_lds_dwordx4 %3, %6 offset:3072\n\t"
          "s_mov_b32 m0, %10\n\ts_nop 0\n\t"
          "global_load_lds_dwordx4 %4, %7\n\tglobal_load_lds_dwordx4 %5, %7 offset:1024\n\t"
          "global_load_lds_dwordx4 %4, %8 offset:2048\n\tglobal_load_lds_dwordx4 %5, %8 offset:3072"
          ::"v"(va), "v"(va + 15360), "v"((va ^ 64) + 2 * 15360), "v"((va ^ 64) + 3 * 15360), "v"(vb), "v"(vb + 15360),
            "s"(A.xB + ko), "s"(A.Bre + ko), "s"(A.Bim + ko - 1024), "s"(dst), "s"(dst + 16384)
          : "memory");
    }
    wait_vm<8>();
    bar();
    wait_vm<0>();
    lds_signal<SW_LD>(U);
  }
  __builtin_amdgcn_s_setprio(2);
  {
    const int nseg = 2 * tm - 8 * wave;
    cf G = cf{0.f, 0.f};
    lds_wait<SW_F1, 2>(U, 1);
    if (nseg > 0) {
      const __amdgpu_buffer_rsrc_t ra = __builtin_amdgcn_make_buffer_rsrc((void*)(ws + WS_AGG), 0, 512 << 10, 0x00020000);
      int o[8];
#pragma unroll
      for (int k = 0; k < 8; ++k) {
        o[k] = ((nseg - 1 - k) * 1024 + tn * 64 + lane) * 16;
        asm volatile("" : "+v"(o[k]));
      }
      u32x4 u[8];
      cf l64 = reinterpret_cast<const cf*>(smem + L_PART)[192 + lane], l128 = reinterpret_cast<const cf*>(smem + L_PART)[384 + lane];
      cf lw = reinterpret_cast<const cf*>(smem + L_GRP)[wave * 64 + lane];
      asm volatile("" : "+v"(l64), "+v"(l128), "+v"(lw));
      lds_wait<SW_PUB, 1>(U, 4);
      __builtin_amdgcn_s_sleep(10);
#pragma nounroll
      for (unsigned spins = 0; spins < SPIN_MAX; ++spins) {
        uint32_t n = 0;
#pragma unroll
        for (int k = 0; k < 8; ++k) u[k] = __builtin_amdgcn_raw_buffer_load_b128(ra, o[k], 0, 16);
#pragma unroll
        for (int k = 0; k < 8; ++k) n += u[k][1] + u[k][3];
        if (__builtin_amdgcn_ballot_w64(n != 2u * (nseg < 8 ? nseg : 8)) == 0) break;
        asm volatile("" ::: "memory");
      }
#pragma unroll
      for (int k = 6; k >= 0; k -= 2)
        G = cmad(G, l128, cmad(cf{u2f(u[k + 1][0]), u2f(u[k + 1][2])}, l64, cf{u2f(u[k][0]), u2f(u[k][2])}));
      G = cmul(G, lw);
    }
    reinterpret_cast<cf*>(smem + L_GRP)[wave * 64 + lane] = G;
    wait_lgkm0();
    lds_signal<SW_GRP>(U);
  }
  if (!T.late) {
    lds_wait<SW_GRP, 2>(U, 4);
    lds_wait<SW_G0E, 2>(U, 4);
    stage_c(A, wave, lane, 0);
    lds_wait<SW_HS, 2>(U, 4);
    stage_c(A, wave, lane, 1);
    stage_c(A, wave, lane, 2);
  }
  lds_wait<SW_G1, 1>(U, 1);
  __builtin_amdgcn_s_setprio(0);
  const int N = T.late ? 32 - NX : NX;
  const char* ab = ws + WS_HS + (size_t)tm * (64 * 8192);
  const uint32_t vh = (wave >> 1) * 262144 + (wave & 1) * 4096 + lane * 16, vc = g1_vc(wave, lane);
#pragma nounroll
  for (int k = 0; k < 3; ++k) {
    const char* pa = ab + g1_kt(T.late, k) * 8192;
    glds2_dev(pa, vh, lds0 + (k + 1) * G0_STAGE);
    glds2_dev(pa + 2048, vh, lds0 + (k + 1) * G0_STAGE + 2048);
  }
  wait_vm<4>();
#pragma nounroll
  for (int k = 3; k < N; ++k) {
    const uint32_t dst = lds0 + ((k + 1) & 3) * G0_STAGE;
    const int kt = g1_kt(T.late, k);
    asm volatile(
        "s_mov_b32 m0, %5\n\ts_waitcnt vmcnt(8)\n\ts_barrier\n\t"
        "global_load_lds_dwordx4 %2, %4 sc1\n\tglobal_load_lds_dwordx4 %2, %4 offset:1024 sc1\n\t"
        "global_load_lds_dwordx4 %2, %4 offset:2048 sc1\n\tglobal_load_lds_dwordx4 %2, %4 offset:3072 sc1\n\t"
        "s_mov_b32 m0, %6\n\ts_nop 0\n\t"
        "global_load_lds_dwordx4 %0, %3\n\tglobal_load_lds_dwordx4 %1, %3 offset:1024\n\t"
        "global_load_lds_dwordx4 %0, %3 offset:2048\n\tglobal_load_lds_dwordx4 %1, %3 offset:3072"
        ::"v"(vc), "v"(vc + 65536 - 1024), "v"(vh), "s"(((kt & 1) ? A.Cim : A.Cre) + (kt >> 1) * 64), "s"(ab + kt * 8192), "s"(dst),
          "s"(dst + 16384)
        : "memory");
  }
  wait_vm<8>();
  bar();
  wait_vm<0>();
  lds_signal<SW_LD>(U);
  if (T.late) {
    const char* ps = ws + WS_PS + (size_t)(bid_x() & 127) * 32768 + wave * 8192;
    lds_wait<SW_PX, 1>(U, 1);
#pragma nounroll
    for (int i = 0; i < 4; ++i) glds2_dev(ps + i * 2048, lane * 16, ldsb + ((29 - NX) & 3) * G0_STAGE + wave * 8192 + i * 2048);
    wait_vm<0>();
    lds_signal<SW_PL>(U);
  }
}

#define W(n) "s_waitcnt lgkmcnt(" #n ")\n\t"
#define M(c, a, b) "v_mfma_f32_16x16x32_bf16 v[" #c ":" #c "+3], v[" #a ":" #a "+3], v[" #b ":" #b "+3], v[" #c ":" #c "+3]\n\t"
#define RB(d, p, o) "ds_read_b128 v[" #d ":" #d "+3], v" #p " offset:" #o "\n\t"
#define RT(d, p, o) "ds_read_b64_tr_b16 v[" #d ":" #d "+1], v" #p " offset:" #o "\n\t"
#define AD(p) "v_add_u32 v" #p ", %[adv], v" #p "\n\t"
#define SA "s_add_i32 %[k], %[k], 1\n\ts_and_b32 %[adv], %[k], 3\n\t"
#define SB "s_cmp_eq_u32 %[adv], 0\n\ts_cselect_b32 %[adv], %[m3], 0x8000\n\t"
#define F(d, p, l) "v_fmac_f32 v[" #d "], v[" #p "], v" #l "\n\t"
#define XA(d, p) F(d, p + 4, 122) F(d + 4, p + 4, 104) F(d + 8, p + 12, 123) F(d + 12, p + 12, 106)
#define XB(d, p) F(d, p, 104) F(d + 4, p, 105) F(d + 8, p + 8, 106) F(d + 12, p + 8, 107)
#define LA0 "v_mbcnt_lo_u32_b32 v108, -1, 0\n\tv_mbcnt_hi_u32_b32 v108, -1, v108\n\t"
#define LA1 "v_and_b32 v108, 15, v108\n\tv_lshl_add_u32 v108, v108, 3, %[pp]\n\t"
#define LAM "ds_read_b64 v[104:105], v108 offset:%[lo]\n\tds_read_b64 v[106:107], v108 offset:%[lo]+128\n\ts_setprio 3\n\t"
#define L16 "ds_read2_b64 v[64:67], v108 offset0:64 offset1:80\n\tds_read2_b64 v[68:71], v108 offset0:128 offset1:144\n\t"
#define NEG "v_xor_b32 v122, 0x80000000, v105\n\tv_xor_b32 v123, 0x80000000, v107\n\t"
#define SIG "v_mov_b32 v109, 4\n\ts_mov_b64 exec, 1\n\tds_add_u32 v109, v109 offset:%[e]\n\ts_mov_b64 exec, -1\n\t"
#define STEP0(C0, CN, H4, H9, H19, H30) \
  W(7) M(0, 64, C0) RB(72, 120, 512) W(7) M(0, 68, C0 + 4) RB(76, 121, 512) \
  W(7) M(4, 64, 80) W(6) M(4, 68, 84) \
  W(5) M(8, 64, 88) SA H4 W(4) M(8, 68, 92) SB \
  W(3) M(12, 64, 96) RB(64, 120, 1024) W(3) M(12, 68, 100) RB(68, 121, 1024) \
  W(3) M(16, 72, C0) W(2) M(16, 76, C0 + 4) AD(122) H9 \
  M(20, 72, 80) RB(CN, 122, 0) M(20, 76, 84) AD(123) \
  M(24, 72, 88) RB(CN + 4, 123, 0) M(24, 76, 92) \
  M(28, 72, 96) RB(72, 120, 1536) M(28, 76, 100) RB(76, 121, 1536) \
  W(5) M(32, 64, C0) W(4) M(32, 68, C0 + 4) \
  M(36, 64, 80) M(36, 68, 84) H19 \
  M(40, 64, 88) AD(120) M(40, 68, 92) AD(121) \
  M(44, 64, 96) RB(64, 120, 0) M(44, 68, 100) RB(68, 121, 0) \
  W(3) M(52, 72, 80) RB(80, 122, 2048) W(3) M(52, 76, 84) RB(84, 123, 2048) \
  M(56, 72, 88) RB(88, 122, 4096) M(56, 76, 92) RB(92, 123, 4096) \
  M(60, 72, 96) RB(96, 122, 6144) M(60, 76, 100) RB(100, 123, 6144) \
  M(48, 72, C0) H30 M(48, 76, C0 + 4)
#define HEAD0(C0) \
  RB(C0, 122, 0) RB(C0 + 4, 123, 0) RB(64, 120, 0) RB(68, 121, 0) RB(80, 122, 2048) RB(84, 123, 2048) RB(88, 122, 4096) RB(92, 123, 4096) RB(96, 122, 6144) RB(100, 123, 6144)
#define STEP1(C0, CN, H4, H9, H19, H30) \
  W(8) M(0, 64, C0) RT(72, 120, 2048) W(6) M(0, 68, C0 + 4) RT(74, 121, 2048) \
  M(4, 64, 80) RT(76, 120, 2560) W(7) M(4, 68, 84) RT(78, 121, 2560) \
  W(7) M(8, 64, 88) SA H4 W(6) M(8, 68, 92) SB \
  W(5) M(12, 64, 96) RT(64, 120, 4096) W(5) M(12, 68, 100) RT(66, 121, 4096) \
  W(4) M(16, 72, C0) RT(68, 120, 4608) W(3) M(16, 76, C0 + 4) RT(70, 121, 4608) \
  M(20, 72, 80) AD(122) H9 M(20, 76, 84) RB(CN, 122, 0) \
  M(24, 72, 88) AD(123) M(24, 76, 92) RB(CN + 4, 123, 0) \
  M(28, 72, 96) RT(72, 120, 6144) M(28, 76, 100) RT(74, 121, 6144) \
  W(6) M(32, 64, C0) RT(76, 120, 6656) W(5) M(32, 68, C0 + 4) RT(78, 121, 6656) \
  M(36, 64, 80) M(36, 68, 84) H19 \
  M(40, 64, 88) AD(120) M(40, 68, 92) AD(121) \
  M(44, 64, 96) RT(64, 120, 0) M(44, 68, 100) RT(66, 121, 0) \
  W(4) M(52, 72, 80) RT(68, 120, 512) RB(80, 122, 2048) W(4) M(52, 76, 84) RT(70, 121, 512) RB(84, 123, 2048) \
  M(56, 72, 88) RB(88, 122, 4096) M(56, 76, 92) RB(92, 123, 4096) \
  M(60, 72, 96) RB(96, 122, 6144) M(60, 76, 100) RB(100, 123, 6144) \
  M(48, 72, C0) H30 M(48, 76, C0 + 4)
#define HEAD1(C0) \
  RB(C0, 122, 0) RB(C0 + 4, 123, 0) RT(64, 120, 0) RT(66, 121, 0) RT(68, 120, 512) RB(80, 122, 2048) RT(70, 121, 512) RB(84, 123, 2048) RB(88, 122, 4096) RB(92, 123, 4096) RB(96, 122, 6144) RB(100, 123, 6144)
#define ROW1(C0) \
  W(8) M(0, 64, C0) RT(72, 120, 2048) W(6) M(0, 68, C0 + 4) RT(74, 121, 2048) \
  M(4, 64, 80) RT(76, 120, 2560) W(7) M(4, 68, 84) RT(78, 121, 2560) \
  W(7) M(8, 64, 88) W(6) M(8, 68, 92) \
  W(5) M(12, 64, 96) W(4) M(12, 68, 100) \
  W(0)
#define TAIL0 \
  W(7) M(0, 64, 112) RB(72, 120, 512) LA0 W(7) M(0, 68, 116) RB(76, 121, 512) LA1 \
  W(7) M(4, 64, 80) LAM W(8) M(4, 68, 84) \
  W(7) M(8, 64, 88) W(6) M(8, 68, 92) \
  W(5) M(12, 64, 96) RB(64, 120, 1024) W(5) M(12, 68, 100) RB(68, 121, 1024) \
  W(5) M(16, 72, 112) W(2) NEG M(16, 76, 116) \
  M(20, 72, 80) XA(1, 0) M(20, 76, 84) XB(1, 0) \
  M(24, 72, 88) XA(2, 1) M(24, 76, 92) XB(2, 1) \
  M(28, 72, 96) RB(72, 120, 1536) XA(3, 2) M(28, 76, 100) RB(76, 121, 1536) XB(3, 2) \
  W(3) M(32, 64, 112) W(2) M(32, 68, 116) \
  M(36, 64, 80) XA(16, 3) M(36, 68, 84) XB(16, 3) \
  M(40, 64, 88) XA(17, 16) M(40, 68, 92) XB(17, 16) \
  M(44, 64, 96) XA(18, 17) M(44, 68, 100) XB(18, 17) \
  W(1) M(48, 72, 112) XA(19, 18) W(0) M(48, 76, 116) SIG XB(19, 18) \
  M(52, 72, 80) L16 XA(32, 19) M(52, 76, 84) XB(32, 19) \
  M(56, 72, 88) XA(33, 32) M(56, 76, 92) XB(33, 32) \
  M(60, 72, 96) XA(34, 33) M(60, 76, 100) XB(34, 33) \
  XA(35, 34) XB(35, 34) "s_nop 3\n\t" XA(48, 35) XB(48, 35) \
  XA(49, 48) XB(49, 48) XA(50, 49) XB(50, 49) \
  XA(51, 50) XB(51, 50) W(0)
#define V2(op, d, a, b) op " v[" #d "], " a ", v[" #b "]\n\t"
#define CV(d, a) "v_cvt_pk_bf16_f32 v[" #d "], v[" #a "], v[" #a "+4]\n\t"
#define ST(d, inc, pol) "buffer_store_dwordx4 v[" #d ":" #d "+3], v123, %[rs], %[sf] offen " pol "\n\ts_add_u32 %[sf], %[sf], " #inc "\n\t"
#define C4(d, a) CV(d, a) CV(d + 1, a + 8) CV(d + 2, a + 1) CV(d + 3, a + 9)
#define SP(d) ST(d, 1024, "sc1")
#define UN(d, p) V2("v_lshlrev_b32", d, "16", p) V2("v_and_b32", d + 1, "0xffff0000", p) V2("v_lshlrev_b32", d + 2, "16", p + 1) V2("v_and_b32", d + 3, "0xffff0000", p + 1)
#define A1(d, a) "v_add_f32 v[" #d "], v[" #a "], v[" #d "]\n\t"
#define AS(d, a, inc) A1(d, a) A1(d + 1, a + 4) A1(d + 2, a + 8) A1(d + 3, a + 12) ST(d, inc, "nt")
#define LE1 "v_lshlrev_b32 v123, 4, v108\n\t"
#define LL1 "v_lshl_add_u32 v122, v108, 4, %[pp]\n\tv_and_b32 v123, 15, v108\n\t"
#define LL2 "v_lshrrev_b32 v108, 4, v108\n\tv_lshlrev_b32 v108, 15, v108\n\tv_lshl_add_u32 v123, v123, 4, v108\n\t"
#define PLW "v_mov_b32 v104, 0\n6:\n\tds_read_b32 v105, v104 offset:%[e]\n\t" W(0) "v_readfirstlane_b32 %[t], v105\n\ts_cmp_lt_u32 %[t], 16\n\ts_cbranch_scc1 6b\n\t"
#define TAIL1E \
  RT(64, 120, 4096) RT(66, 121, 4096) RT(68, 120, 4608) RT(70, 121, 4608) M(16, 72, 112) LA0 LE1 M(16, 76, 116) \
  M(20, 72, 80) M(20, 76, 84) \
  M(24, 72, 88) M(24, 76, 92) C4(104, 0) SP(104) \
  M(28, 72, 96) RT(72, 120, 6144) M(28, 76, 100) RT(74, 121, 6144) C4(108, 2) SP(108) \
  W(4) M(32, 64, 112) RT(76, 120, 6656) W(3) M(32, 68, 116) RT(78, 121, 6656) \
  M(36, 64, 80) M(36, 68, 84) \
  M(40, 64, 88) M(40, 68, 92) C4(0, 16) SP(0) \
  M(44, 64, 96) M(44, 68, 100) C4(4, 18) SP(4) \
  W(2) M(48, 72, 112) W(0) M(48, 76, 116) \
  M(52, 72, 80) M(52, 76, 84) \
  M(56, 72, 88) M(56, 76, 92) C4(8, 32) SP(8) \
  M(60, 72, 96) M(60, 76, 100) C4(12, 34) SP(12) \
  "s_nop 7\n\t" C4(16, 48) C4(20, 50) \
  SP(16) SP(20)
#define TAIL1L \
  RT(64, 120, 4096) RT(66, 121, 4096) RT(68, 120, 4608) RT(70, 121, 4608) M(16, 72, 112) LA0 LL1 M(16, 76, 116) LL2 \
  M(20, 72, 80) PLW RB(104, 122, 0) M(20, 76, 84) \
  M(24, 72, 88) W(0) UN(108, 104) M(24, 76, 92) AS(108, 0, 8192) \
  M(28, 72, 96) RT(72, 120, 6144) RB(108, 122, 1024) UN(104, 106) M(28, 76, 100) RT(74, 121, 6144) AS(104, 1, 8192) \
  M(32, 64, 112) RT(76, 120, 6656) W(2) UN(104, 108) M(32, 68, 116) RT(78, 121, 6656) AS(104, 2, 8192) \
  M(36, 64, 80) RB(104, 122, 2048) UN(108, 110) M(36, 68, 84) AS(108, 3, 106496) \
  M(40, 64, 88) RB(108, 122, 3072) M(40, 68, 92) W(1) UN(0, 104) \
  M(44, 64, 96) AS(0, 16, 8192) M(44, 68, 100) UN(4, 106) \
  M(48, 72, 112) AS(4, 17, 8192) RB(104, 122, 4096) M(48, 76, 116) W(1) UN(8, 108) \
  M(52, 72, 80) AS(8, 18, 8192) M(52, 76, 84) UN(12, 110) \
  M(56, 72, 88) AS(12, 19, 106496) RB(108, 122, 5120) M(56, 76, 92) W(1) UN(16, 104) \
  M(60, 72, 96) AS(16, 32, 8192) M(60, 76, 100) UN(20, 106) \
  AS(20, 33, 8192) RB(104, 122, 6144) W(1) UN(24, 108) AS(24, 34, 8192) UN(28, 110) \
  AS(28, 35, 106496) RB(108, 122, 7168) "s_nop 7\n\t" W(1) UN(32, 104) AS(32, 48, 8192) UN(36, 106) \
  AS(36, 49, 8192) W(0) UN(40, 108) AS(40, 50, 8192) UN(44, 110) \
  AS(44, 51, 106496)
#define ASK "v_mov_b32 v112, 0\n\tds_read_b32 v112, v112 offset:%[o]\n\t"
#define CHK "v_readfirstlane_b32 %[t], v112\n\ts_cmp_lt_u32 %[t], %[cnt]\n\t"
#define LOOP(X)                                                                                                           \
  "1:\n\t" STEP##X(104, 112, , , , "s_barrier\n\t") "5:\n\t"                                                                \
  STEP##X(112, 104, , , "s_cmp_lg_u32 %[k], %[k1]\n\t", "s_cbranch_scc0 2f\n\ts_barrier\n2:\n\t") "s_cbranch_scc1 1b\n\t" \
  STEP##X(104, 112, ASK, CHK "s_cbranch_scc0 4f\n3:\n\t" ASK W(0) CHK "s_cbranch_scc1 3b\n4:\n\t", , )
#define ACC \
  "+{v[0:3]}"(acc[0][0]), "+{v[4:7]}"(acc[0][1]), "+{v[8:11]}"(acc[0][2]), "+{v[12:15]}"(acc[0][3]), \
  "+{v[16:19]}"(acc[1][0]), "+{v[20:23]}"(acc[1][1]), "+{v[24:27]}"(acc[1][2]), "+{v[28:31]}"(acc[1][3]), \
  "+{v[32:35]}"(acc[2][0]), "+{v[36:39]}"(acc[2][1]), "+{v[40:43]}"(acc[2][2]), "+{v[44:47]}"(acc[2][3]), \
  "+{v[48:51]}"(acc[3][0]), "+{v[52:55]}"(acc[3][1]), "+{v[56:59]}"(acc[3][2]), "+{v[60:63]}"(acc[3][3]), \
  "+{v120}"(pa0), "+{v121}"(pa1), "+{v122}"(pb0), "+{v123}"(pb1)
#define CLOB \
  "scc", "memory", "v64", "v65", "v66", "v67", "v68", "v69", "v70", "v71", "v72", "v73", "v74", "v75", "v76", "v77", "v78", "v79", \
  "v80", "v81", "v82", "v83", "v84", "v85", "v86", "v87", "v88", "v89", "v90", "v91", "v92", "v93", "v94", "v95", "v96", "v97", \
  "v98", "v99", "v100", "v101", "v102", "v103", "v104", "v105", "v106", "v107", "v108", "v109", "v110", "v111", "v112", "v113", \
  "v114", "v115", "v116", "v117", "v118", "v119"
__device__ __forceinline__ void run0(f32x4 (&acc)[4][4], uint32_t pa0, uint32_t pb0, uint32_t pp, u32x4& lam, u32x4& l16, u32x4& l32) {
  uint32_t pa1 = pa0 ^ 64, pb1 = pb0 ^ 64, t;
  int k = 0, adv;
  asm volatile("" : ACC);
  bar_lds();
  asm volatile(HEAD0(104) LOOP(0) TAIL0
               : ACC, [k] "+s"(k), [adv] "=&s"(adv), [t] "=&s"(t)
               : [k1] "n"(14), [m3] "s"(-3 * G0_STAGE), [cnt] "n"(16), [o] "n"(SW_LD * 4), [e] "n"(SW_G0E * 4 - 4), [pp] "s"(pp), [lo] "n"(L_PRM - L_PART)
               : CLOB);
  asm volatile("" : "={v[104:107]}"(lam), "={v[64:67]}"(l16), "={v[68:71]}"(l32));
  __builtin_amdgcn_sched_barrier(0);
}
__device__ __forceinline__ void run1(f32x4 (&acc)[4][4], uint32_t pa0, uint32_t pb0, int n, uint32_t pp, u32x4 rs, uint32_t sf) {
  uint32_t pa1 = pa0 ^ 64, pb1 = pb0 ^ 64, t;
  int k = 1, adv;
  asm volatile("" : ACC);
  bar_lds();
#define REST \
  LOOP(1) ROW1(112) "s_cmp_lg_u32 %[pp], 0\n\ts_cbranch_scc1 7f\n\t" TAIL1E "s_branch 8f\n7:\n\t" TAIL1L "8:" \
  : ACC, [k] "+s"(k), [adv] "=&s"(adv), [t] "=&s"(t), [sf] "+s"(sf) \
  : [k1] "s"(n - 1), [m3] "s"(-3 * G0_STAGE), [cnt] "n"(32), [o] "n"(SW_LD * 4), [e] "n"(SW_PL * 4), [pp] "s"(pp), [rs] "s"(rs) \
  : CLOB
  if (NX & 1) asm volatile(HEAD1(112) "s_branch 5f\n\t" REST);
  else asm volatile(HEAD1(104) REST);
}
#undef W
#undef M

__device__ __forceinline__ void mfma_role(const Args& A, char* smem, int wave, int lane) {
  char* const ws = A.ws;
  const Tile T = tile_map();
  const int tm = T.tm, tn = T.tn;
  lds_u32* const sw = (lds_u32*)(smem + L_SYNC);
  const uint32_t U = (uint32_t)(size_t)(sw + 1);
  const int wr = wave >> 1, wc = wave & 1;
  int fr = lane & 15, fq = lane >> 4;
  {
    if (T.late) bar_lds();
  }
  if (wave == 3) {
    wait_vm<0>();
    const u16* raw = reinterpret_cast<const u16*>(smem + L_PRM + 512);
    uint32_t pr = raw[lane], pi = raw[64 + lane], pd = raw[128 + lane];
    asm volatile("" : "+v"(pr), "+v"(pi), "+v"(pd)::"memory");
    auto expf_ = [](float x) {
      const float t = x * 1.44269504088896341f;
      const float r = __builtin_fmaf(x, 1.44269504088896341f, -t) + x * 1.92596299112661746e-8f;
      const float e = __builtin_amdgcn_exp2f(t);
      return __builtin_fmaf(e * r, 0.693147180559945309f, e);
    };
    const float lr = u2f(pr << 16), li = u2f(pi << 16);
    const float dt = rbf(expf_(u2f(pd << 16)));
    const float zr = dt * lr, zi = dt * li;
    const float e = expf_(zr);
    const float rh = zi * 0.159154936671257019f;
    const float rf = (rh - __builtin_rintf(rh)) + __builtin_fmaf(zi, 6.42063832653e-9f, __builtin_fmaf(zi, 0.159154936671257019f, -rh));
    const cf lam = cf{e * __builtin_amdgcn_cosf(rf), e * __builtin_amdgcn_sinf(rf)};
    const float nr = lam.x - 1.0f, ni = lam.y;
    const float den = 2.0f / (lr * lr + li * li);
    cf* prm = reinterpret_cast<cf*>(smem + L_PRM);
    prm[lane] = lam;
    prm[64 + lane] = cf{(nr * lr + ni * li) * den, (ni * lr - nr * li) * den};
    cf lp[10];
    lp[0] = cmul(lam, lam);
#pragma unroll
    for (int k = 1; k < 10; ++k) lp[k] = cmul(lp[k - 1], lp[k - 1]);
    cf* pt = reinterpret_cast<cf*>(smem + L_PART) + lane;
    pt[64] = lp[3], pt[128] = lp[4], pt[192] = lp[5], pt[256] = lp[0], pt[320] = lp[1], pt[384] = lp[6];
    pt = reinterpret_cast<cf*>(smem + L_GRP) + lane;
    pt[0] = cf{1.f, 0.f}, pt[64] = lp[8], pt[128] = lp[9], pt[192] = cmul(lp[8], lp[9]);
  }
  __builtin_amdgcn_s_setprio(1);
  {
    f32x4 acc[4][4];
#pragma unroll
    for (int m = 0; m < 4; ++m)
#pragma unroll
      for (int n = 0; n < 4; ++n) acc[m][n] = Z4;
    const cf* prm = reinterpret_cast<const cf*>(smem + L_PRM) + wc * 32;
    cf* part = reinterpret_cast<cf*>(smem + L_PART) + wc * 32;
    const cf* grp = reinterpret_cast<const cf*>(smem + L_GRP);
    cf lam[2], l16[2], l32[2], s[2][16];
    {
      u32x4 t0, t1, t2;
      run0(acc, lds_base(smem) + (wr * 64 + (fr >> 2) * 16 + (fr & 3)) * 128 + (fq ^ (fr & 7)) * 16,
           lds_base(smem) + (128 + wc * 64 + fr) * 128 + (fq ^ (fr & 7)) * 16, lds_base(smem) + L_PART + wc * 256, t0, t1, t2);
#pragma unroll
      for (int jp = 0; jp < 2; ++jp) {
        lam[jp] = cf{u2f(t0[2 * jp]), u2f(t0[2 * jp + 1])};
        l16[jp] = cf{u2f(t1[2 * jp]), u2f(t1[2 * jp + 1])};
        l32[jp] = cf{u2f(t2[2 * jp]), u2f(t2[2 * jp + 1])};
      }
      lane = lane_id();
      fr = lane & 15;
      fq = lane >> 4;
#pragma unroll
      for (int i = 0; i < 32; ++i) s[i >> 4][i & 15] = cf{acc[(i >> 2) & 3][2 * (i >> 4)][i & 3], acc[(i >> 2) & 3][2 * (i >> 4) + 1][i & 3]};
    }
    const uint64_t m1 = 0xFFFF0000FFFF0000ull, m2 = 0xFFFFFFFF00000000ull;
    cf pe[2], a01[2], wg[2];
#pragma unroll
    for (int jp = 0; jp < 2; ++jp) {
      const u32x2 ex = __builtin_amdgcn_permlane16_swap(f2u(s[jp][15].x), f2u(s[jp][15].x), false, false);
      const u32x2 ey = __builtin_amdgcn_permlane16_swap(f2u(s[jp][15].y), f2u(s[jp][15].y), false, false);
      pe[jp] = cf{u2f(ex[0]), u2f(ey[0])};
      const cf ap = cmad(pe[jp], l16[jp], cf{u2f(ex[1]), u2f(ey[1])});
      const u32x2 hx = __builtin_amdgcn_permlane32_swap(f2u(ap.x), f2u(ap.x), false, false);
      const u32x2 hy = __builtin_amdgcn_permlane32_swap(f2u(ap.y), f2u(ap.y), false, false);
      a01[jp] = cf{u2f(hx[0]), u2f(hy[0])};
      wg[jp] = cmad(a01[jp], l32[jp], cf{u2f(hx[1]), u2f(hy[1])});
    }
    {
      const cf ws_ = sel_c(m1, wg[1], wg[0]);
      if (lane < 32)
        __builtin_amdgcn_raw_buffer_store_b128(u32x4{f2u(ws_.x), 1u, f2u(ws_.y), 1u},
                                               __builtin_amdgcn_make_buffer_rsrc((void*)(ws + WS_AGG), 0, 512 << 10, 0x00020000),
                                               ((2 * tm + wr) * 1024 + tn * 64 + wc * 32 + lane) * 16, 0, 16);
      if (wr == 0) {
        part[lane & 31] = ws_;
        wait_lgkm0();
      }
      lds_signal<SW_PUB>(U);
      if (wr) lds_wait<SW_PUB, 0>(U, 4);
    }
    __builtin_amdgcn_s_setprio(0);
    cf l2[2], l3[2], l4[2], c[2], pw[2];
#pragma unroll
    for (int jp = 0; jp < 2; ++jp) {
      const cf cof = prm[64 + jp * 16 + fr];
      l2[jp] = part[256 + jp * 16 + fr];
      l4[jp] = part[320 + jp * 16 + fr];
      const cf p1 = sel_c(m1, l16[jp], cf{1.f, 0.f});
      cf cc = sel_c(m1, pe[jp], cf{0.f, 0.f});
      cc = sel_c(m2, cmad(p1, a01[jp], cc), cc);
      cf p = sel_c(m2, cmul(p1, l32[jp]), p1);
      if (wr) {
        cc = cmad(p, part[jp * 16 + fr], cc);
        p = cmul(p, part[192 + jp * 16 + fr]);
      }
      pw[jp] = cmul(p, cof);
      c[jp] = cmul(cc, cof);
      l3[jp] = cmul(l2[jp], lam[jp]);
      asm volatile("" : "+v"(pw[jp]), "+v"(c[jp]), "+v"(l3[jp]), "+v"(l4[jp]));
#pragma unroll
      for (int i = 0; i < 16; ++i) {
        s[jp][i] = cmul(s[jp][i], cof);
        asm volatile("" : "+v"(s[jp][i]));
      }
    }
    __builtin_amdgcn_s_setprio(3);
    lds_wait<SW_GRP, 1>(U, 4);
    const __amdgpu_buffer_rsrc_t rs = __builtin_amdgcn_make_buffer_rsrc((void*)(ws + WS_HS), 0, 8 << 20, 0x00020000);
    const int hb = ((tm * 2 + wr) * 32 + tn * 2) * 8192 + fq * 2048 + (wc * 32 + fr) * 16;
#pragma unroll
    for (int jp = 0; jp < 2; ++jp) {
      const int pl = wc * 32 + jp * 16 + fr;
      const cf hin = (grp[192 + pl] + grp[128 + pl]) + (grp[64 + pl] + grp[pl]);
      cf t = cmad(pw[jp], hin, c[jp]);
#pragma unroll
      for (int k = 0; k < 4; ++k) {
        s[jp][4 * k] = cmad(t, lam[jp], s[jp][4 * k]);
        s[jp][4 * k + 1] = cmad(t, l2[jp], s[jp][4 * k + 1]);
        s[jp][4 * k + 2] = cmad(t, l3[jp], s[jp][4 * k + 2]);
        t = cmul(t, l4[jp]);
        s[jp][4 * k + 3] = s[jp][4 * k + 3] + t;
      }
#pragma unroll
      for (int h = 0; h < 2; ++h) {
        u32x4 re, im;
#pragma unroll
        for (int i = 0; i < 4; ++i) {
          re[i] = pk_bf(s[jp][8 * h + 2 * i].x, s[jp][8 * h + 2 * i + 1].x);
          im[i] = pk_bf(-s[jp][8 * h + 2 * i].y, -s[jp][8 * h + 2 * i + 1].y);
        }
        __builtin_amdgcn_raw_buffer_store_b128(re, rs, (hb ^ (h * 64)) + jp * 256 + h * 1024, 0, 16);
        __builtin_amdgcn_raw_buffer_store_b128(im, rs, (hb ^ (h * 64)) + jp * 256 + h * 1024 + 8192, 0, 16);
      }
    }
  }
  lds_signal<SW_HS>(U);
  if (T.late) {
    __builtin_amdgcn_s_setprio(0);
    for (int i = 0; i < 1; ++i) bar();
    publish(ws, 4 * tm + 2, lane);
    for (int i = 0; i < 3; ++i) bar();
  } else {
    publish(ws, 4 * tm + (T.nt >> 2), lane);
    lds_signal<SW_HD>(U);
    if (wave == 0) poll_count<SW_G1>(ws, 4 * tm + 1, 16, U);
  }
}

__device__ __forceinline__ void gemm1(const Args& A, char* smem, int wave, int lane) {
  char* const ws = A.ws;
  const Tile T = tile_map();
  const int tm = T.tm, nt = T.nt;
  const uint32_t U = 4;
  const int wr = wave >> 1, wc = wave & 1, fr = lane & 15, fq = lane >> 4;
  f32x4 acc[4][4];
  if (T.late) {
    const u32x2* px = reinterpret_cast<const u32x2*>(A.xB + (tm * 128 + wr * 64 + fq * 4) * 1024 + nt * 128 + wc * 64) + fr;
    const u32x2 dw = reinterpret_cast<const u32x2*>(A.Dsk + nt * 128 + wc * 64)[fr];
    u32x2 xw[16];
#pragma unroll
    for (int i = 0; i < 16; ++i) xw[i] = px[((i >> 2) * 16 + (i & 3)) * 256];
#pragma unroll
    for (int i = 0; i < 32; ++i) {
      const uint32_t a = xw[i >> 1][i & 1], d = dw[i & 1];
      const uint32_t p = pk_bf(u2f(a << 16) * u2f(d << 16), u2f(a & 0xFFFF0000u) * u2f(d & 0xFFFF0000u));
      acc[i >> 3][(i & 1) * 2][(i >> 1) & 3] = u2f(p << 16);
      acc[i >> 3][(i & 1) * 2 + 1][(i >> 1) & 3] = u2f(p & 0xFFFF0000u);
    }
#pragma unroll
    for (int m = 0; m < 4; ++m) asm volatile("" : "+v"(acc[m][0]), "+v"(acc[m][1]), "+v"(acc[m][2]), "+v"(acc[m][3]));
    lds_wait<SW_G0E, 2>(U, 4);
#pragma nounroll
    for (int k = 0; k < 3; ++k) {
      if (k == 2) lds_wait<SW_GRP, 2>(U, 4);
      stage_c(A, wave, lane, k);
    }
    wait_vm<0>();
  } else {
#pragma unroll
    for (int i = 0; i < 16; ++i) acc[i >> 2][i & 3] = Z4;
  }
  {
    const uint64_t base = T.late ? (uint64_t)A.out : (uint64_t)(ws + WS_PS);
    __builtin_amdgcn_s_setprio(1);
    run1(acc, lds_base(smem) + G0_STAGE + wr * 8192 + ((fr >> 1) & 1) * 1024 + (8 * fq + (fr >> 2) + (fr & 2) * 2) * 16 + (fr & 1) * 8,
         lds_base(smem) + G0_STAGE + 16384 + (wc * 64 + fr) * 128 + (fq ^ (fr & 7)) * 16, T.late ? 32 - NX : NX,
         T.late ? lds_base(smem) + ((29 - NX) & 3) * G0_STAGE + wave * 8192 : 0,
         u32x4{(uint32_t)base, (uint32_t)(base >> 32) & 0xFFFFu, 16u << 20, 0x00020000u},
         T.late ? ((tm * 128 + wr * 64) * 2048 + 1024 + nt * 128 + wc * 64) * 4 : bid_x() * 32768 + wave * 8192);
  }
  if (!T.late) {
    lane = lane_id();
    lds_signal<SW_PL>(U);
    publish(ws, 64 + bid_x(), lane);
    warm_dev(smem, ws + WS_PS, bid_x() * 32768 + wave * 8192 + lane * 128);
  }
}

}

extern "C" __global__ __launch_bounds__(1024, 4) void k_all(const uint32_t* x2, const unsigned short* w_r, const unsigned short* w_i,
                                                           const unsigned short* xB, const unsigned short* Lre,
                                                           const unsigned short* Lim, const unsigned short* Bre,
                                                           const unsigned short* Bim, const unsigned short* Cre,
                                                           const unsigned short* Cim, const unsigned short* ldt,
                                                           const unsigned short* Dsk, float* out, char* ws) {
  __shared__ __attribute__((aligned(256))) char smem_[LDS_BYTES];
  char* const smem = smem_ + 256;
  const Args A{x2, w_r, w_i, xB, Lre, Lim, Bre, Bim, Cre, Cim, ldt, Dsk, out, ws};
  const int tid = tid_x(), lane = tid & 63;
  int wave = __builtin_amdgcn_readfirstlane(tid >> 6);
  const uint32_t ldsb = lds_base(smem);
  {
    const int o = lane * 128 + (lane >> 5) * ((bid_x() << 9) & (35192 > 36864 ? 0xF000 : 0x7000));
    uint32_t t0, t1, t2, t3;
    asm volatile(
        "s_cmp_ge_u32 %[w], 8\n\ts_cbranch_scc1 8f\n\t"
        "s_load_dwordx2 s[64:65], %[ka], 0\n\ts_load_dwordx2 s[66:67], %[ka], 24\n\ts_load_dwordx4 s[68:71], %[ka], 48\n\t"
        "s_cmp_lg_u32 %[w], 0\n\ts_cbranch_scc1 1f\n\ts_getpc_b64 s[72:73]\n\ts_add_u32 s72, s72, k_all@rel32@lo+4\n\t"
        "s_addc_u32 s73, s73, k_all@rel32@hi+12\n\ts_mov_b32 m0, %[scr]\n\ts_nop 0\n\tglobal_load_lds_dword %[o], s[72:73]\n"
        "1:\n\tv_lshrrev_b32 %0, 3, %[l]\n\tv_and_b32 %1, 7, %[l]\n\ts_lshl_b32 s72, %[w], 12\n\t"
        "s_cmp_lt_u32 %[b], 128\n\ts_cbranch_scc1 2f\n\t"
        "s_add_u32 m0, s72, %[s3]\n\ts_lshl_b32 s72, %[w], 19\n\ts_and_b32 s73, %[b], 7\n\ts_lshl_b32 s73, s73, 11\n\ts_add_u32 s72, s72, s73\n\t"
        "s_lshl_b32 s73, %[b], 4\n\ts_and_b32 s73, s73, 0x780\n\ts_add_u32 s72, s72, s73\n\t"
        "v_lshrrev_b32 %2, 1, %0\n\tv_xor_b32 %1, %1, %2\n\tv_lshlrev_b32 %1, 4, %1\n\tv_lshl_add_u32 %0, %0, 14, %1\n\t"
        "v_add_u32 %0, s72, %0\n\tv_xor_b32 %1, 64, %0\n\tv_add_u32 %1, 130048, %1\n\tv_add_u32 %2, 260096, %0\n\t"
        "v_add_u32 %3, 260096, %1\n\ts_waitcnt lgkmcnt(0)\n\t"
        "global_load_lds_dwordx4 %0, s[64:65] nt\n\tglobal_load_lds_dwordx4 %1, s[64:65] offset:1024 nt\n\t"
        "global_load_lds_dwordx4 %2, s[64:65] offset:2048 nt\n\tglobal_load_lds_dwordx4 %3, s[64:65] offset:3072 nt\n\ts_branch 9f\n"
        "2:\n\ts_add_u32 m0, s72, 256\n\ts_mul_i32 s73, %[b], 1280\n\ts_and_b32 s73, s73, 0x600\n\t"
        "s_cmp_ge_u32 %[w], 4\n\ts_cbranch_scc1 3f\n\t"
        "s_lshl_b32 s72, %[b], 17\n\ts_and_b32 s72, s72, 0x3c0000\n\ts_add_u32 s73, s73, s72\n\ts_lshl_b32 s72, %[w], 16\n\t"
        "s_add_u32 s73, s73, s72\n\tv_and_b32 %2, 3, %0\n\tv_xor_b32 %1, %1, %2\n\tv_lshlrev_b32 %1, 4, %1\n\t"
        "v_lshl_add_u32 %0, %0, 11, %1\n\tv_add_u32 %0, s73, %0\n\tv_add_u32 %1, 15360, %0\n\tv_xor_b32 %2, 64, %0\n\t"
        "v_add_u32 %3, 46080, %2\n\tv_add_u32 %2, 30720, %2\n\ts_waitcnt lgkmcnt(0)\n\t"
        "global_load_lds_dwordx4 %0, s[66:67]\n\tglobal_load_lds_dwordx4 %1, s[66:67] offset:1024\n\t"
        "global_load_lds_dwordx4 %2, s[66:67] offset:2048\n\tglobal_load_lds_dwordx4 %3, s[66:67] offset:3072\n\ts_branch 9f\n"
        "3:\n\ts_and_b32 s72, %[b], 1\n\ts_lshl_b32 s72, s72, 19\n\ts_add_u32 s73, s73, s72\n\ts_lshl_b32 s72, %[b], 12\n\t"
        "s_and_b32 s72, s72, 0x60000\n\ts_add_u32 s73, s73, s72\n\ts_and_b32 s72, %[w], 3\n\ts_lshl_b32 s72, s72, 15\n\t"
        "s_add_u32 s73, s73, s72\n\tv_xor_b32 %1, %1, %0\n\tv_lshlrev_b32 %1, 4, %1\n\tv_lshl_add_u32 %0, %0, 11, %1\n\t"
        "v_add_u32 %0, s73, %0\n\tv_add_u32 %1, 15360, %0\n\ts_waitcnt lgkmcnt(0)\n\t"
        "global_load_lds_dwordx4 %0, s[68:69]\n\tglobal_load_lds_dwordx4 %1, s[68:69] offset:1024\n\ts_add_u32 m0, m0, 2048\n\ts_nop 0\n\t"
        "global_load_lds_dwordx4 %0, s[70:71]\n\tglobal_load_lds_dwordx4 %1, s[70:71] offset:1024\n\ts_branch 9f\n"
        "8:\n\ts_cmp_lg_u32 %[w], 15\n\ts_cbranch_scc1 9f\n\ts_setprio 3\n\t"
        "s_load_dwordx4 s[64:67], %[ka], 32\n\ts_load_dwordx2 s[68:69], %[ka], 80\n\t"
        "s_and_b32 s72, %[b], 1\n\ts_lshl_b32 s72, s72, 9\n\ts_lshl_b32 s73, %[b], 2\n\ts_and_b32 s73, s73, 0x180\n\ts_add_u32 s72, s72, s73\n\t"
        "s_and_b32 s73, %[b], 128\n\ts_lshl_b32 s73, s73, 3\n\ts_add_u32 s72, s72, s73\n\tv_lshl_add_u32 %0, %[l], 2, s72\n\t"
        "s_mov_b32 m0, %[prm]\n\ts_mov_b32 exec_hi, 0\n\ts_waitcnt lgkmcnt(0)\n\tglobal_load_lds_dword %0, s[64:65]\n\t"
        "s_add_u32 m0, m0, 128\n\ts_nop 0\n\tglobal_load_lds_dword %0, s[66:67]\n\ts_add_u32 m0, m0, 128\n\ts_nop 0\n\t"
        "global_load_lds_dword %0, s[68:69]\n\ts_mov_b32 exec_hi, -1\n\ts_setprio 0\n9:"
        : "=&v"(t0), "=&v"(t1), "=&v"(t2), "=&v"(t3)
        : [l] "v"(lane), [o] "v"(o < 35192 - 16 ? o : 35192 - 16), [w] "s"(wave), [b] "s"(bid_x()),
          [ka] "s"(__builtin_amdgcn_kernarg_segment_ptr()), [scr] "n"(256 + L_SCR2), [s3] "n"(256 + 3 * G0_STAGE), [prm] "n"(256 + L_PRM + 512)
        : "s64", "s65", "s66", "s67", "s68", "s69", "s70", "s71", "s72", "s73", "scc", "memory");
  }
  if (wave < 8) {
  } else if (wave < 12) {
    const int wc = (bid_x() >> 3) & 15;
    if (0) __builtin_amdgcn_s_sleep(0);
    if (bid_x() >= 128 && (wc & 7) != 3 && wave != 10)
      warm_r(A, smem, lane, wave == 11 ? (wc & 8) | 3 : wc, wave == 11 ? (wc & 7) - ((wc & 7) > 3) : wave - 8);
  } else if (wave == 13) {
    warm1(smem, Dsk, (lane & 15) * 128);
  }
  asm volatile("" : "+s"(wave));
  bool g1 = false;
  if (__builtin_expect(wave >= 12, 0)) {
    if (wave == 12) *(volatile lds_u32*)((lds_u32*)(smem + L_SYNC) + lane) = 0;
    mfma_role(A, smem, wave - 12, lane);
    g1 = bid_x() < 128;
  } else if (__builtin_expect(wave >= 8, 0)) {
    loader_role(A, smem, wave - 8, lane);
  } else {
    g1 = fft_role(A, smem, wave, lane);
  }
  if (g1) gemm1(A, smem, wave & 3, (int)__builtin_amdgcn_mbcnt_hi(~0u, __builtin_amdgcn_mbcnt_lo(~0u, 0u)));
}
#if defined(__HIP_DEVICE_COMPILE__) && __HIP_DEVICE_COMPILE__
#pragma clang attribute pop
#endif

extern "C" void kernel_launch(void* const* d_in, const int* in_sizes, int n_in, void* d_out, int out_size, void* d_ws,
                              size_t ws_size, hipStream_t stream) {
  typedef const unsigned short* P;
  k_all<<<256, 1024, 0, stream>>>((const uint32_t*)d_in[0], (P)d_in[1], (P)d_in[2], (P)d_in[3], (P)d_in[4], (P)d_in[5], (P)d_in[6],
                                  (P)d_in[7], (P)d_in[8], (P)d_in[9], (P)d_in[10], (P)d_in[11], (float*)d_out, (char*)d_ws);
}
```

```cpp
#include <hip/hip_runtime.h>
#include <stdint.h>

namespace {

#if defined(__HIP_DEVICE_COMPILE__) && __HIP_DEVICE_COMPILE__
#pragma clang attribute push(__attribute__((target("no-packed-fp32-ops"))), apply_to = function)
#endif
__device__ __forceinline__ int tid_x() { return __builtin_amdgcn_workitem_id_x(); }
__device__ __forceinline__ int bid_x() { return __builtin_amdgcn_workgroup_id_x(); }

typedef float cf __attribute__((ext_vector_type(2)));
typedef short bf16x8 __attribute__((ext_vector_type(8)));
typedef short s16x4 __attribute__((ext_vector_type(4)));
typedef _Float16 f16x8 __attribute__((ext_vector_type(8)));
typedef _Float16 f16x2 __attribute__((ext_vector_type(2)));
typedef __bf16 b16x2 __attribute__((ext_vector_type(2)));
typedef float f32x4 __attribute__((ext_vector_type(4)));
typedef uint32_t u32x4 __attribute__((ext_vector_type(4)));
typedef uint32_t u32x2 __attribute__((ext_vector_type(2)));
typedef unsigned short u16;
typedef unsigned long long u64;
typedef __attribute__((address_space(3))) uint32_t lds_u32;

__device__ __forceinline__ float u2f(uint32_t u) { return __builtin_bit_cast(float, u); }
__device__ __forceinline__ uint32_t f2u(float f) { return __builtin_bit_cast(uint32_t, f); }
__device__ __forceinline__ float bf2f(u16 b) { return u2f((uint32_t)b << 16); }
__device__ __forceinline__ float rbf(float f) { return bf2f(__builtin_bit_cast(u16, (__bf16)f)); }
__device__ __forceinline__ uint32_t pk_bf(float a, float b) {
  const b16x2 h = b16x2{(__bf16)a, (__bf16)b};
  return __builtin_bit_cast(uint32_t, h);
}
__device__ __forceinline__ uint32_t pk_h(float a, float b) {
  const f16x2 h = f16x2{(_Float16)a, (_Float16)b};
  return __builtin_bit_cast(uint32_t, h);
}
__device__ __forceinline__ cf fin(cf v) {
  float x = v.x, y = v.y;
  asm("" : "+v"(x), "+v"(y));
  return cf{x, y};
}
__device__ __forceinline__ cf cmul(cf a, cf b) {
  return cf{__builtin_fmaf(a.x, b.x, -(a.y * b.y)), __builtin_fmaf(a.x, b.y, a.y * b.x)};
}
__device__ __forceinline__ cf cmad(cf a, cf b, cf c) {
  return cf{__builtin_fmaf(a.x, b.x, __builtin_fmaf(-a.y, b.y, c.x)), __builtin_fmaf(a.x, b.y, __builtin_fmaf(a.y, b.x, c.y))};
}
__device__ __forceinline__ cf cisr(int k, float inv_n) {
  const float r = (float)k * inv_n;
  return cf{__builtin_amdgcn_cosf(r), -__builtin_amdgcn_sinf(r)};
}
template <int N> __device__ __forceinline__ cf cis(int k) { return cisr(k, 1.0f / N); }
template <int N> __device__ __forceinline__ void wait_vm() { asm volatile("s_waitcnt vmcnt(%0)" ::"n"(N) : "memory"); }
__device__ __forceinline__ void wait_lgkm0() { asm volatile("s_waitcnt lgkmcnt(0)" ::: "memory"); }
__device__ __forceinline__ void glds2_dev(const void* sb, uint32_t v0, uint32_t lds_dst) {
  uint32_t keep;
  asm volatile(
      "s_mov_b32 %0, m0\n\ts_mov_b32 m0, %3\n\ts_nop 0\n\tglobal_load_lds_dwordx4 %1, %2 sc1\n\t"
      "global_load_lds_dwordx4 %1, %2 offset:1024 sc1\n\ts_mov_b32 m0, %0"
      : "=&s"(keep)
      : "v"(v0), "s"(sb), "s"(lds_dst)
      : "memory");
}
__device__ __forceinline__ void gdev2(const void* sb, uint32_t v0, uint32_t v1, uint32_t lds_dst) {
  uint32_t keep;
  asm volatile(
      "s_mov_b32 %0, m0\n\ts_mov_b32 m0, %4\n\ts_nop 0\n\tglobal_load_lds_dwordx4 %1, %3 sc1\n\t"
      "global_load_lds_dwordx4 %2, %3 offset:1024 sc1\n\ts_mov_b32 m0, %0"
      : "=&s"(keep)
      : "v"(v0), "v"(v1), "s"(sb), "s"(lds_dst)
      : "memory");
}
__device__ __forceinline__ void glds4(const void* sb0, const void* sb1, uint32_t v0, uint32_t v1, uint32_t v2, uint32_t v3,
                                      uint32_t lds_dst) {
  uint32_t keep;
  asm volatile(
      "s_mov_b32 %0, m0\n\ts_mov_b32 m0, %7\n\ts_nop 0\n\tglobal_load_lds_dwordx4 %1, %5\n\t"
      "global_load_lds_dwordx4 %2, %5 offset:1024\n\tglobal_load_lds_dwordx4 %3, %6 offset:2048\n\t"
      "global_load_lds_dwordx4 %4, %6 offset:3072\n\ts_mov_b32 m0, %0"
      : "=&s"(keep)
      : "v"(v0), "v"(v1), "v"(v2), "v"(v3), "s"(sb0), "s"(sb1), "s"(lds_dst)
      : "memory");
}
__device__ __forceinline__ constexpr uint32_t lds_base(char*) { return 256; }
__device__ __forceinline__ void bar() {
  __builtin_amdgcn_sched_barrier(0);
  asm volatile("" ::: "memory");
  __builtin_amdgcn_s_barrier();
  asm volatile("" ::: "memory");
  __builtin_amdgcn_sched_barrier(0);
}
__device__ __forceinline__ void bar_lds() {
  asm volatile("s_waitcnt lgkmcnt(0)" ::: "memory");
  bar();
}
template <int W> __device__ __forceinline__ void lds_signal(uint32_t U) {
  asm volatile("s_mov_b64 exec, 1\n\tds_add_u32 %0, %0 offset:%1\n\ts_mov_b64 exec, -1" ::"v"(U), "n"(W * 4 - 4) : "memory");
}
template <int W> __device__ __forceinline__ uint32_t lds_peek(uint32_t U) {
  return __builtin_amdgcn_readfirstlane(*(volatile lds_u32*)(size_t)(U + (W * 4 - 4)));
}
template <int W, int SLEEP> __device__ __forceinline__ void lds_wait(uint32_t U, uint32_t n) {
  uint32_t v, c;
  if (SLEEP)
    asm volatile(
        "s_branch 2f\n1:\n\ts_sleep %5\n2:\n\tds_read_b32 %0, %2 offset:%4\n\ts_waitcnt lgkmcnt(0)\n\tv_readfirstlane_b32 %1, %0\n\t"
        "s_cmp_lt_u32 %1, %3\n\ts_cbranch_scc1 1b"
        : "=&v"(v), "=&s"(c)
        : "v"(U), "s"(n * U), "n"(W * 4 - 4), "n"(SLEEP)
        : "memory", "scc");
  else
    asm volatile(
        "1:\n\tds_read_b32 %0, %2 offset:%4\n\ts_waitcnt lgkmcnt(0)\n\tv_readfirstlane_b32 %1, %0\n\t"
        "s_cmp_lt_u32 %1, %3\n\ts_cbranch_scc1 1b"
        : "=&v"(v), "=&s"(c)
        : "v"(U), "s"(n * U), "n"(W * 4 - 4)
        : "memory", "scc");
}
__device__ __forceinline__ int lane_id() {
  int l;
  asm volatile("v_mbcnt_lo_u32_b32 %0, -1, 0\n\tv_mbcnt_hi_u32_b32 %0, -1, %0" : "=v"(l));
  return l;
}
__device__ __forceinline__ uint32_t wave_max_u32(uint32_t v) {
  asm volatile(
      "s_nop 1\n\tv_max_u32_dpp %0, %0, %0 quad_perm:[1,0,3,2] row_mask:0xf bank_mask:0xf\n\t"
      "s_nop 1\n\tv_max_u32_dpp %0, %0, %0 quad_perm:[2,3,0,1] row_mask:0xf bank_mask:0xf\n\t"
      "s_nop 1\n\tv_max_u32_dpp %0, %0, %0 row_half_mirror row_mask:0xf bank_mask:0xf\n\t"
      "s_nop 1\n\tv_max_u32_dpp %0, %0, %0 row_mirror row_mask:0xf bank_mask:0xf\n\t"
      "s_nop 1\n\tv_max_u32_dpp %0, %0, %0 row_bcast:15 row_mask:0xa bank_mask:0xf\n\t"
      "s_nop 1\n\tv_max_u32_dpp %0, %0, %0 row_bcast:31 row_mask:0xc bank_mask:0xf\n\ts_nop 1"
      : "+v"(v));
  return (uint32_t)__builtin_amdgcn_readlane((int)v, 63);
}
__device__ __forceinline__ float sel_f(uint64_t m, float a, float b) {
  float r;
  asm("v_cndmask_b32_e64 %0, %1, %2, %3" : "=v"(r) : "v"(b), "v"(a), "s"(m));
  return r;
}
__device__ __forceinline__ cf sel_c(uint64_t m, cf a, cf b) { return cf{sel_f(m, a.x, b.x), sel_f(m, a.y, b.y)}; }
__device__ __forceinline__ u64 ld_dev(const u64* p) { return __hip_atomic_load(p, __ATOMIC_RELAXED, __HIP_MEMORY_SCOPE_AGENT); }
__device__ __forceinline__ void st_dev(u64* p, u64 v) { __hip_atomic_store(p, v, __ATOMIC_RELAXED, __HIP_MEMORY_SCOPE_AGENT); }
__device__ __forceinline__ void st_dev(uint32_t* p, uint32_t v) {
  __hip_atomic_store(p, v, __ATOMIC_RELAXED, __HIP_MEMORY_SCOPE_AGENT);
}
__device__ __forceinline__ f32x4 mfma_h(u32x4 a, u32x4 b, f32x4 c) {
  return __builtin_amdgcn_mfma_f32_16x16x32_f16(__builtin_bit_cast(f16x8, a), __builtin_bit_cast(f16x8, b), c, 0, 0, 0);
}
__device__ __forceinline__ f32x4 mfma_b(u32x4 a, u32x4 b, f32x4 c) {
  return __builtin_amdgcn_mfma_f32_16x16x32_bf16(__builtin_bit_cast(bf16x8, a), __builtin_bit_cast(bf16x8, b), c, 0, 0, 0);
}
struct PQ {
  u32x4 p, q;
};
constexpr f32x4 Z4 = f32x4{0.f, 0.f, 0.f, 0.f};
constexpr unsigned SPIN_MAX = 1u << 20;

constexpr size_t WS_EPOCH = 0;
constexpr size_t WS_F1 = 4096;
constexpr size_t WS_P2 = 8192;
constexpr size_t WS_HF = 1u << 19;
constexpr size_t WS_AGG = 1u << 20;
constexpr size_t WS_ZB = 2u << 20;
constexpr size_t WS_ZC = 8u << 20;
constexpr size_t WS_HS = 18u << 20;
constexpr size_t WS_PS = 26u << 20;

constexpr int NX = 13;
constexpr int G0_STAGE = 32768;
constexpr int L_Z = 0;
constexpr int L_X = 131072;
constexpr int L_TB = L_X + 16384;
constexpr int L_WS = L_TB + 4096;
constexpr int L_PART = L_WS + 4096;
constexpr int L_GRP = L_PART + 3584;
constexpr int L_RS = L_GRP + 2048;
constexpr int L_SYNC = -256;
constexpr int L_PRM = L_RS + 1024;
constexpr int L_SCR2 = L_PRM + 1024;
constexpr int LDS_BYTES = L_SCR2 + 256 + 256;
enum { SW_GRP = 2, SW_PUB, SW_WMAX, SW_XMAX, SW_HD, SW_G1, SW_FS, SW_G0E, SW_HS, SW_LD, SW_RS, SW_F1, SW_PX, SW_FR, SW_PL, SW_P2 };

struct Args {
  const uint32_t* x2;
  const u16 *w_r, *w_i, *xB, *Lre, *Lim, *Bre, *Bim, *Cre, *Cim, *ldt, *Dsk;
  float* out;
  char* ws;
};

struct Tile {
  int tm, tn, nt, late;
};
__device__ __forceinline__ Tile tile_map() {
  const int bid = bid_x(), late = bid >> 7, cq = (bid & 1) * 4 + ((bid >> 5) & 3);
  return Tile{(bid >> 1) & 15, late * 8 + cq, cq, late};
}
__device__ __forceinline__ u32x4 lds4(const void* p) { return *reinterpret_cast<const u32x4*>(p); }
__device__ __forceinline__ u32x4 ldsa(uint32_t a) { return *(__attribute__((address_space(3))) u32x4*)(size_t)a; }
__device__ __forceinline__ void lds_cf2(const void* p, cf& a, cf& b) {
  const f32x4 v = *reinterpret_cast<const f32x4*>(p);
  a = cf{v[0], v[1]};
  b = cf{v[2], v[3]};
}
template <int W> __device__ __forceinline__ void group_sync(uint32_t U, uint32_t n) {
  wait_lgkm0();
  lds_signal<W>(U);
  lds_wait<W, 0>(U, n);
}
__device__ __forceinline__ int k_rot() { return (bid_x() & 6) * 2 + (bid_x() & 1) * 8; }
__device__ __forceinline__ void stage0(const Args& A, char* smem, int wave, int lane, int kt) {
  const Tile T = tile_map();
  const int prow = lane >> 3;
  const uint32_t va = (T.tm * 128 + wave * 32 + prow) * 2048 + ((lane & 7) ^ (prow & 3)) * 16;
  const uint32_t vb = (T.tn * 64 + wave * 16 + prow) * 2048 + ((lane & 7) ^ prow) * 16;
  const uint32_t dst = lds_base(smem) + wave * 4096 + (kt & 3) * G0_STAGE;
  kt = (kt + k_rot()) & 15;
  glds4(A.xB + kt * 64, A.xB + kt * 64, va, va + 15360, (va ^ 64) + 2 * 15360, (va ^ 64) + 3 * 15360, dst);
  glds4(A.Bre + kt * 64, A.Bim + kt * 64 - 1024, vb, vb + 15360, vb, vb + 15360, dst + 16384);
}
__device__ __forceinline__ int g1_kt(int late, int s) {
  const int e = late ? s - 2 : s, o = 2 * tile_map().nt, r = late ? s - 10 : s + 14 - NX;
  return (unsigned)e < 8u ? ((e + 6) & 7) + (late ? 0 : 8) : (e < 0 ? o + s : (r < o ? r : r + 2)) + 16;
}
__device__ __forceinline__ uint32_t g1_vc(int wave, int lane) {
  return (tile_map().nt * 128 + (wave >> 1) * 64 + (wave & 1) * 2 + (lane >> 3) * 4) * 2048 + ((lane & 7) ^ (lane >> 3)) * 16;
}
__device__ __forceinline__ void stage_c(const Args& A, int wave, int lane, int s) {
  const int kt = g1_kt(tile_map().late, s);
  const u16* src = ((kt & 1) ? A.Cim : A.Cre) + (kt >> 1) * 64;
  const uint32_t vc = g1_vc(wave, lane);
  glds4(src, src, vc, vc + 65536 - 1024, vc, vc + 65536 - 1024, 256 + ((s + 1) & 3) * G0_STAGE + 16384 + wave * 4096);
}
__device__ __forceinline__ void warm1(char* smem, const void* base, uint32_t off) {
  uint32_t keep;
  asm volatile("s_mov_b32 %0, m0\n\ts_mov_b32 m0, %3\n\ts_nop 0\n\tglobal_load_lds_dword %1, %2\n\ts_mov_b32 m0, %0"
               : "=&s"(keep)
               : "v"(off), "s"(base), "s"(lds_base(smem) + L_SCR2)
               : "memory");
}
__device__ __forceinline__ void warm_dev(char* smem, const void* base, uint32_t off) {
  uint32_t keep;
  asm volatile("s_mov_b32 %0, m0\n\ts_mov_b32 m0, %3\n\ts_nop 0\n\tglobal_load_lds_dword %1, %2 sc1\n\ts_mov_b32 m0, %0"
               : "=&s"(keep)
               : "v"(off), "s"(base), "s"(lds_base(smem) + L_SCR2)
               : "memory");
}
typedef short s16x2 __attribute__((ext_vector_type(2)));
__device__ __forceinline__ uint32_t pmax(uint32_t m, uint32_t u) {
  return __builtin_bit_cast(uint32_t, __builtin_elementwise_max(__builtin_bit_cast(s16x2, m), __builtin_bit_cast(s16x2, u & 0x7FFF7FFFu)));
}
template <int W> __device__ __forceinline__ void lds_max(uint32_t U, uint32_t m) {
  const uint32_t h = m << 16;
  m = h > m ? h : m;
  asm volatile(
      "s_nop 1\n\tv_max_u32_dpp %0, %0, %0 quad_perm:[1,0,3,2] row_mask:0xf bank_mask:0xf\n\t"
      "s_nop 1\n\tv_max_u32_dpp %0, %0, %0 quad_perm:[2,3,0,1] row_mask:0xf bank_mask:0xf\n\t"
      "s_nop 1\n\tv_max_u32_dpp %0, %0, %0 row_half_mirror row_mask:0xf bank_mask:0xf\n\t"
      "s_mov_b64 exec, %3\n\tds_max_u32 %1, %0 offset:%2\n\ts_mov_b64 exec, -1"
      : "+v"(m)
      : "v"(U), "n"(W * 4 - 4), "s"(0x0101010101010101ull)
      : "memory");
}
__device__ __forceinline__ void warm_r(const Args& A, char* smem, int lane, int c, int i) {
  const int bid = bid_x(), tm = ((bid >> 1) & 3) + (c & 3) * 4, nt = (bid & 1) * 4 + (c >> 2);
  uint64_t px = (uint64_t)A.xB, pbr = (uint64_t)A.Bre, pbi = (uint64_t)A.Bim;
  asm volatile("" : "+s"(px), "+s"(pbr), "+s"(pbi));
  px += (tm * 128 + (nt & 3) * 32 + (lane & 31)) * 2048;
  const uint64_t pb = ((lane & 16) ? pbi : pbr) + (nt * 64 + (c & 3) * 16 + (lane & 15)) * 2048;
  const int hi = lane >> 5;
  const bool k2 = i == 4 || i == 6;
  const int sl = i < 3 ? i + 1 : (k2 ? i - 4 : i + 1) + hi;
  const uint64_t pfp = ((k2 || (i < 3 && hi)) ? pb + (k2 ? 1 << 20 : 0) : px) + ((sl + k_rot()) & 15) * 128;
  uint32_t keep;
  asm volatile("s_mov_b32 %0, m0\n\ts_mov_b32 m0, %2\n\ts_nop 0\n\tglobal_load_lds_dword %1, off\n\ts_mov_b32 m0, %0"
               : "=&s"(keep)
               : "v"(pfp), "s"(lds_base(smem) + L_SCR2)
               : "memory");
}
__device__ __forceinline__ uint32_t pk_mul_h(uint32_t a, uint32_t b) {
  return __builtin_bit_cast(uint32_t, __builtin_bit_cast(f16x2, a) * __builtin_bit_cast(f16x2, b));
}
__device__ __forceinline__ u32x2 lds_tr(uint32_t a) {
  return __builtin_bit_cast(u32x2, __builtin_amdgcn_ds_read_tr16_b64_v4i16((__attribute__((address_space(3))) s16x4*)(size_t)a));
}
__device__ __forceinline__ void sta4(uint32_t a, u32x4 v) { *(__attribute__((address_space(3))) u32x4*)(size_t)a = v; }
template <int P> __device__ __forceinline__ void prio(bool on) {
  __builtin_amdgcn_sched_barrier(0);
  if (on) __builtin_amdgcn_s_setprio(P);
  __builtin_amdgcn_sched_barrier(0);
}
__device__ __forceinline__ void row_head(u32x4 (&x)[2][2], const PQ& m16, const PQ& m1, const cf (&tw)[2][4], bool on) {
  f32x4 dr[2][2], di[2][2];
#pragma unroll
  for (int k = 0; k < 2; ++k) {
    const f32x4 ur = mfma_h(x[k][0], m16.p, Z4), ui = mfma_h(x[k][0], m16.q, Z4);
    const u32x4 xn = x[k][1] ^ 0x80008000u;
    dr[k][0] = mfma_h(x[k][1], m1.p, ur), di[k][0] = mfma_h(x[k][1], m1.q, ui);
    dr[k][1] = mfma_h(xn, m1.p, ur), di[k][1] = mfma_h(xn, m1.q, ui);
  }
  prio<0>(on);
#pragma unroll
  for (int i = 0; i < 16; ++i) {
    const int k = i >> 3, t = (i >> 2) & 1, r = i & 3;
    const cf v = fin(cmul(cf{dr[k][t][r], di[k][t][r]}, tw[t][r]));
    dr[k][t][r] = v.x, di[k][t][r] = v.y;
  }
  prio<1>(on);
#pragma unroll
  for (int i = 0; i < 16; ++i) x[i >> 3][(i >> 2) & 1][i & 3] = pk_h(dr[i >> 3][(i >> 2) & 1][i & 3], di[i >> 3][(i >> 2) & 1][i & 3]);
}
template <int W, bool B = false> __device__ __forceinline__ void poll_count(char* ws, int idx, uint32_t n, uint32_t U) {
  uint32_t c0, c1, c2, t, lim = SPIN_MAX;
#define ASK(c) "global_atomic_add " c ", %5, %5, %6 sc0\n\t"
#define CHK(c) "s_waitcnt vmcnt(%10)\n\tv_readfirstlane_b32 %3, " c "\n\ts_cmp_ge_u32 %3, %7\n\ts_cbranch_scc1 2f\n\t" ASK(c)
  asm volatile(
      "s_mov_b64 exec, 1\n\t" ASK("%0") "s_sleep 4\n\t" ASK("%1") "s_sleep 4\n\t" ASK("%2") ".if %11\n\ts_barrier\n.endif\n1:\n\t" CHK("%0") CHK("%1") CHK("%2")
      "s_sub_u32 %4, %4, 1\n\ts_cbranch_scc0 1b\n2:\n\tds_add_u32 %8, %8 offset:%9\n\ts_mov_b64 exec, -1\n\ts_waitcnt vmcnt(0)"
      : "=&v"(c0), "=&v"(c1), "=&v"(c2), "=&s"(t), "+s"(lim)
      : "v"(0), "s"(ws + WS_HF + idx * 128), "s"(n), "v"(U), "n"(W * 4 - 4), "n"(2), "n"(B)
      : "memory", "scc");
#undef ASK
#undef CHK
}
__device__ __forceinline__ bool fft_role(const Args& A, char* smem, int f, int lane) {
  char* const ws = A.ws;
  const Tile T = tile_map();
  const int bid = bid_x(), li = bid & 127;
  int l15 = lane & 15, q = lane >> 4;
  lds_u32* const sw = (lds_u32*)(smem + L_SYNC);
  const uint32_t U = (uint32_t)(size_t)(sw + 1), ldsb = lds_base(smem);
  const __amdgpu_buffer_rsrc_t rzb = __builtin_amdgcn_make_buffer_rsrc((void*)(ws + WS_ZB), 0, 16 << 20, 0x00020000);
  auto fsync = [&](int n) { group_sync<SW_FS>(U, 8 * n); };
  auto bars = [&](int n) {
#pragma nounroll
    for (int i = 0; i < n; ++i) bar();
  };
  u64 epoch = 0;
  if (f == 0) epoch = ld_dev((const u64*)(ws + WS_EPOCH));
  if (f == 7) {
    const __amdgpu_buffer_rsrc_t ra = __builtin_amdgcn_make_buffer_rsrc((void*)(ws + WS_AGG), 0, 512 << 10, 0x00020000);
    __builtin_amdgcn_raw_buffer_store_b128(u32x4{0u, 0u, 0u, 0u}, ra, (T.tm * 2048 + T.tn * 64 + lane) * 16, 0, 16);
    __builtin_amdgcn_raw_buffer_store_b128(u32x4{0u, 0u, 0u, 0u}, ra, (T.tm * 2048 + 1024 + T.tn * 64 + lane) * 16, 0, 16);
    if (!T.late && lane < (T.tn ? 1 : 4)) st_dev((uint32_t*)(ws + WS_HF + (lane ? 4 * T.tm + lane - 1 : 64 + li) * 128), 0u);
    if (lane == 0) st_dev((uint32_t*)(ws + WS_P2) + bid, 0u);
  }
  __builtin_amdgcn_sched_barrier(0);
  auto ks_of = [&](int slot) {
    const int w = slot & 7, k = bid ? bid + 256 * w : (w < 4 ? 256 * w : 256 * w - 896);
    return slot < 8 ? k : (k ? 2048 - k : 1024);
  };
  {
    const int e = f & 3, n = 4 * q + e;
    uint32_t* tb = reinterpret_cast<uint32_t*>(smem + L_TB) + lane * 4;
    uint32_t* tx = reinterpret_cast<uint32_t*>(smem + L_X) + lane * 4;
    cf c = cis<256>(n * l15 * (f < 4 ? 16 : 1));
    if (f < 4) {
      c = c * 0.25f;
      tb[e] = pk_h(c.x, -c.y);
      tb[256 + e] = pk_h(c.y, c.x);
      if (T.late) {
        const uint32_t ph = pk_bf(c.x, -c.y), qh = pk_bf(c.y, c.x);
        const float lr = c.x - u2f(ph << 16), lo = c.y - u2f(qh << 16);
        tx[e] = ph;
        tx[256 + e] = qh;
        tx[512 + e] = pk_bf(lr, -lo);
        tx[768 + e] = pk_bf(lo, lr);
      } else {
        const cf d = cis<32>((2 * n + 1) * l15) * 0.25f;
        tx[e] = pk_h(d.x, -d.y);
        tx[256 + e] = pk_h(d.y, d.x);
      }
    } else {
      *reinterpret_cast<cf*>(tb + 512 + (e >> 1) * 256 + (e & 1) * 2) = c;
    }
    if (!T.late) {
      *reinterpret_cast<cf*>(tx + 512 + (f >> 1) * 256 + (f & 1) * 2) = cis<512>(n * (16 * (f >> 2) + l15)) * 0.5f;
      const cf w = cis<1024>(l15 + 16 * (f >> 2) + 32 * n);
      *reinterpret_cast<cf*>(tx + 1536 + (f >> 1) * 256 + (f & 1) * 2) = cf{w.y, -w.x};
      const int slot = f < 4 ? l15 : n, u = f < 4 ? q >> 1 : l15 & 1, j = f < 4 ? n & 7 : l15 >> 1;
      const cf g = cis<2048>(j * ks_of(slot)) * (((bid ? slot >> 3 : (slot >> 2) & 1) == u) ? 0.5f : 0.f);
      const uint32_t cj = slot < 8 ? 0u : 0x80000000u;
      tx[2560 + (f >> 2) * 512 + e] = pk_h(g.x, -g.y) ^ (f < 4 ? 0u : cj);
      tx[2816 + (f >> 2) * 512 + e] = pk_h(g.y, g.x) ^ (f < 4 ? cj | cj >> 16 : cj);
    }
  }
  if (!T.late) {
    if (f == 0) wait_vm<1>(); else if (f == 7) wait_vm<4>(); else wait_vm<0>();
  }
  bar_lds();
  PQ m16, m1, m8;
  cf tw[2][4];
  u32x4 wv[4];
  uint32_t pay = 0;
  m16.p = lds4(smem + L_TB + lane * 16), m16.q = lds4(smem + L_TB + 1024 + lane * 16);
  if (T.late) {
    const char* tab = smem + L_X + lane * 16;
    const uint32_t xl = ldsb + 3 * G0_STAGE + f * 4096 + lane * 16;
    const uint32_t xc = ldsb + 3 * G0_STAGE + (l15 + 64 * q) * 128 + (f ^ (l15 >> 1)) * 16;
    PQ mh, ml;
    mh.p = lds4(tab), mh.q = lds4(tab + 1024), ml.p = lds4(tab + 2048), ml.q = lds4(tab + 3072);
    lds_cf2(smem + L_TB + 2048 + lane * 16, tw[0][0], tw[0][1]), lds_cf2(smem + L_TB + 3072 + lane * 16, tw[0][2], tw[0][3]);
    if (f == 0) wait_vm<1>(); else if (f == 7) wait_vm<3>(); else wait_vm<0>();
    {
      uint32_t mx[2] = {0, 0};
#pragma unroll
      for (int i = 0; i < 4; ++i) wv[i] = ldsa(xl + i * 1024);
#pragma unroll
      for (int i = 0; i < 16; ++i) mx[i & 1] = pmax(mx[i & 1], wv[i >> 2][i & 3]);
      lds_max<SW_XMAX>(U, pmax(mx[0], mx[1]));
    }
    fsync(1);
#pragma unroll
    for (int e = 0; e < 4; ++e) wv[e] = ldsa(xc + e * 2048);
    int ecu = (int)(lds_peek<SW_XMAX>(U) >> 23);
    ecu = ecu < 26 ? 26 : ecu;
    pay = (uint32_t)ecu;
    float sc = u2f((uint32_t)(257 - ecu) << 23);
    asm volatile("" : "+v"(sc));
#pragma unroll
    for (int r = 0; r < 4; ++r) tw[0][r] = tw[0][r] * sc;
    f32x4 dr[4], di[4];
#pragma unroll
    for (int c = 0; c < 4; ++c) {
      const u32x4 x = u32x4{wv[0][c], wv[1][c], wv[2][c], wv[3][c]};
      dr[c] = mfma_b(x, mh.p, mfma_b(x, ml.p, Z4));
      di[c] = mfma_b(x, mh.q, mfma_b(x, ml.q, Z4));
    }
#pragma unroll
    for (int i = 0; i < 16; ++i) {
      const cf v = fin(cmul(cf{dr[i >> 2][i & 3], di[i >> 2][i & 3]}, tw[0][i & 3]));
      dr[i >> 2][i & 3] = v.x, di[i >> 2][i & 3] = v.y;
    }
#pragma unroll
    for (int c = 0; c < 4; ++c) {
      const u32x4 a = u32x4{pk_h(dr[c][0], di[c][0]), pk_h(dr[c][1], di[c][1]), pk_h(dr[c][2], di[c][2]), pk_h(dr[c][3], di[c][3])};
      dr[c] = mfma_h(m16.p, a, Z4);
      di[c] = mfma_h(m16.q, a, Z4);
    }
#pragma unroll
    for (int r = 0; r < 4; ++r)
      sta4(xc + r * 2048, u32x4{pk_h(dr[0][r], di[0][r]), pk_h(dr[1][r], di[1][r]), pk_h(dr[2][r], di[2][r]), pk_h(dr[3][r], di[3][r])});
    fsync(2);
#pragma unroll
    for (int i = 0; i < 4; ++i) wv[i] = ldsa(xl + i * 1024);
    const int zo = (32 * f + (lane >> 3)) * 16384 + (li & 7) * 2048 + (li >> 3) * 128 + ((lane & 7) ^ q) * 16;
    asm volatile("" : "+v"(wv[0]), "+v"(wv[1]), "+v"(wv[2]), "+v"(wv[3]));
#pragma unroll
    for (int i = 0; i < 4; ++i) __builtin_amdgcn_raw_buffer_store_b128(wv[i], rzb, (zo ^ ((i & 1) * 64)) + i * 131072, 0, 16);
    bar();
  } else {
    const char* tab = smem + L_X + lane * 16;
    m1.p = lds4(tab), m1.q = lds4(tab + 1024), m8.p = lds4(tab + 10240), m8.q = lds4(tab + 11264);
#pragma unroll
    for (int i = 0; i < 4; ++i) lds_cf2(tab + 2048 + i * 1024, tw[i >> 1][(i & 1) * 2], tw[i >> 1][(i & 1) * 2 + 1]);
  }
  wait_vm<0>();
  asm volatile("" : "+v"(epoch));
  const u64 tag = ((epoch + 1) & 0x7FFFFFFFFFFFull) | 0x800000000000ull;
  u64 f1 = (tag << 16) | (u64)pay;
  asm volatile("" : "+v"(f1));
  bar();
  if (f == 0 && lane == 0) st_dev((u64*)(ws + WS_F1) + bid, f1);
  int nb = f == 0 && !T.late ? 10 : 11;
  if (T.late && f >= 6) {
    uint64_t pbr = (uint64_t)A.Bre, pbi = (uint64_t)A.Bim;
    asm volatile("" : "+s"(pbr), "+s"(pbi));
    uint64_t pf = ((lane & 16) ? pbi : pbr) + (T.tn * 64 + (T.tm >> 2) * 16 + l15) * 2048 + ((2 * f + (lane >> 5) + k_rot()) & 15) * 128;
    asm volatile("" : "+v"(pf));
    bars(f - 3);
    nb -= f - 3;
    __builtin_amdgcn_s_sleep(5);
    uint32_t keep;
    asm volatile("s_mov_b32 %0, m0\n\ts_mov_b32 m0, %2\n\ts_nop 0\n\tglobal_load_lds_dword %1, off\n\ts_mov_b32 m0, %0"
                 : "=&s"(keep)
                 : "v"(pf), "s"(ldsb + L_SCR2)
                 : "memory");
  }
  bars(nb);
  int zo = (q < 2 ? bid : (bid ? 256 - bid : 128)) * 16384 + (q & 1) * 8192 + f * 256 + l15 * 16;
  asm volatile("" : "+v"(zo));
  uint32_t ex = 0, e2 = 0, e3 = 0;
  if (f == 0) {
    u64 v[4];
    auto poll1 = [&]() {
#pragma unroll
      for (int i = 0; i < 4; ++i) v[i] = ld_dev((const u64*)(ws + WS_F1) + lane + 64 * i);
    };
    __builtin_amdgcn_s_sleep(5);
    poll1();
    if (!T.late) bar();
    bar();
    __builtin_amdgcn_s_setprio(2);
#pragma nounroll
    for (unsigned spins = 0; spins < SPIN_MAX; ++spins) {
      bool ok = true;
#pragma unroll
      for (int i = 0; i < 4; ++i) ok &= (v[i] >> 16) == tag;
      if (__builtin_amdgcn_ballot_w64(!ok) == 0) break;
      __builtin_amdgcn_s_sleep(16);
      poll1();
    }
    lds_signal<SW_F1>(U);
    e2 = (uint32_t)v[2] & 255u, e3 = (uint32_t)v[3] & 255u;
    asm volatile("" : "+v"(e2), "+v"(e3));
  } else {
    if (T.late && f >= 4) {
      __builtin_amdgcn_s_sleep(6);
      warm1(smem, (f & 1) ? A.Cre : A.Cim, (T.nt * 32 + T.tm * 2 + ((f >> 1) & 1)) * 8192 + lane * 128);
    }
    bar();
  }
  u32x4 zi[4];
  if (!T.late) {
    if (f) lds_wait<SW_F1, 1>(U, 1);
#pragma unroll
    for (int e = 0; e < 4; ++e) zi[e] = __builtin_amdgcn_raw_buffer_load_b128(rzb, zo + e * 2048, 0, 16);
  }
  if (f == 0) {
    ex = wave_max_u32(e2 > e3 ? e2 : e3);
    const float s2 = __builtin_amdgcn_ldexpf(1.0f, (int)e2 - (int)ex), s3 = __builtin_amdgcn_ldexpf(1.0f, (int)e3 - (int)ex);
    uint32_t* rs = reinterpret_cast<uint32_t*>(smem + L_RS) + lane;
    rs[0] = pk_h(s2, s2);
    rs[64] = pk_h(s3, s3);
    wait_lgkm0();
    lds_signal<SW_RS>(U);
    __builtin_amdgcn_s_setprio(0);
  }
  if (f == 0 && lane == 0) {
    if (T.late) st_dev((uint32_t*)(ws + WS_P2) + bid, 256u);
    if (bid == 0) st_dev((u64*)(ws + WS_EPOCH), epoch + 1);
  }
  if ((bid | f) == 0) __builtin_amdgcn_s_setprio(3);
  if (!T.late) {
    const __amdgpu_buffer_rsrc_t rr = __builtin_amdgcn_make_buffer_rsrc((void*)A.w_r, 0, 2048 * 1026, 0x00020000);
    const __amdgpu_buffer_rsrc_t ri = __builtin_amdgcn_make_buffer_rsrc((void*)A.w_i, 0, 2048 * 1026, 0x00020000);
    uint32_t sr, si;
    auto wload = [&](int ka) {
      const int oa = ka * 1026, ob = ((2048 - ka) & 2047) * 1026, o1 = (ka & 1) ? oa - 2 : ob, o0 = (ka & 1) ? ob + 2 : oa;
      wv[0] = __builtin_amdgcn_raw_buffer_load_b128(rr, lane * 16, o1, 2);
      wv[1] = __builtin_amdgcn_raw_buffer_load_b128(ri, lane * 16, o1, 2);
      wv[2] = __builtin_amdgcn_raw_buffer_load_b128(rr, lane * 16, o0, 2);
      wv[3] = __builtin_amdgcn_raw_buffer_load_b128(ri, lane * 16, o0, 2);
      const int so = (int)f2u(sel_f(1, u2f(ob), sel_f(2, u2f(oa + 1024), u2f(o1 + 1024))));
      sr = __builtin_amdgcn_raw_buffer_load_b16(rr, so, 0, 2);
      si = __builtin_amdgcn_raw_buffer_load_b16(ri, so, 0, 2);
    };
    const bool two = (bid | f) == 0;
    u32x4 w2r, w2i;
    uint32_t s2r, s2i;
    if (two) {
      wload(1024);
      w2r = wv[2], w2i = wv[3], s2r = sr, s2i = si;
    }
    wload(ks_of(f));
    const uint32_t row = ldsb + L_Z + f * 2048;
    uint32_t wa = row, wb = two ? ldsb + L_X + 14336 : row + 16384;
    uint32_t ra = row + q * 512 + ((l15 >> 2) ^ f ^ ((q & 1) << 2)) * 16 + (l15 & 3) * 4;
    uint32_t w1 = ((bid & 1) ? wa : wb) + lane * 16, w0 = ((bid & 1) ? wb : wa) + lane * 16;
    uint32_t za = ldsb + L_Z + l15 * 2048 + f * 256 + ((4 * q) ^ (l15 & 7) ^ ((f & 2) << 1)) * 16;
    asm volatile("" : "+v"(ra), "+v"(w1), "+v"(w0), "+v"(za));
    __builtin_amdgcn_sched_barrier(0);
    lds_wait<SW_RS, 1>(U, 1);
    {
      const u32x4 rs = lds4(smem + L_RS + f * 64 + (l15 >> 3) * 32 + (q & 1) * 16);
      u32x4 o[4];
      asm volatile("" : "+v"(zi[0]), "+v"(zi[1]), "+v"(zi[2]), "+v"(zi[3]));
#pragma unroll
      for (int c = 0; c < 4; ++c) {
        u32x4 a;
#pragma unroll
        for (int e = 0; e < 4; ++e) a[e] = pk_mul_h(zi[e][c], rs[e]);
        const f32x4 re = mfma_h(a, m8.p, Z4), im = mfma_h(a, m8.q, Z4);
#pragma unroll
        for (int r = 0; r < 4; ++r) o[r][c] = pk_h(re[r], im[r]);
      }
#pragma unroll
      for (int r = 0; r < 4; ++r) sta4(za ^ (r * 16), o[r]);
    }
    __builtin_amdgcn_sched_barrier(0);
    uint32_t wsp;
    {
      auto shift = [](u32x4& w, uint32_t last) {
        const uint32_t nx = (uint32_t)__builtin_amdgcn_update_dpp((int)last, (int)(uint32_t)w[0], 0x130, 0xF, 0xF, false);
        w = u32x4{__builtin_amdgcn_alignbit(w[1], w[0], 16), __builtin_amdgcn_alignbit(w[2], w[1], 16),
                  __builtin_amdgcn_alignbit(w[3], w[2], 16), __builtin_amdgcn_alignbit(nx, w[3], 16)};
      };
      uint32_t mx[4] = {0, 0, 0, 0};
#pragma unroll
      for (int i = 0; i < 8; ++i) mx[i & 1] = pmax(mx[i & 1], wv[2 + (i >> 2)][i & 3]);
      shift(wv[0], sr), shift(wv[1], si);
      wsp = sr | si << 16;
      if (two) {
        sta4(ldsb + L_WS + lane * 16, w2r), sta4(ldsb + L_WS + 1024 + lane * 16, w2i);
#pragma unroll
        for (int i = 0; i < 4; ++i) mx[0] = pmax(mx[0], w2r[i]), mx[1] = pmax(mx[1], w2i[i]);
        shift(w2r, s2r), shift(w2i, s2i);
        sta4(ldsb + L_WS + 2048 + lane * 16, w2r), sta4(ldsb + L_WS + 3072 + lane * 16, w2i);
        wsp = (uint32_t)__builtin_amdgcn_update_dpp((int)wsp, (int)(s2r | s2i << 16), 0x114, 0xF, 0x2, false);
      }
#pragma unroll
      for (int i = 0; i < 8; ++i) mx[2 + (i & 1)] = pmax(mx[2 + (i & 1)], wv[i >> 2][i & 3]);
      lds_max<SW_WMAX>(U, pmax(pmax(pmax(mx[0], wsp), mx[1]), pmax(mx[2], mx[3])));
    }
    fsync(1);
    prio<1>(!two);
    u32x4 x[2][2];
    auto rows = [&]() {
#pragma unroll
      for (int i = 0; i < 16; ++i) x[i >> 3][(i >> 2) & 1][i & 3] = *(lds_u32*)(size_t)((ra ^ ((i & 4) * 16)) + (i & 3) * 128 + (i >> 3) * 16384);
    };
    rows();
    pay = lds_peek<SW_WMAX>(U) >> 23;
    pay = pay < 26u ? 26u : pay;
    float fs = __builtin_amdgcn_ldexpf(0.25f, 125 - (int)pay);
    if (two) x[1][0] = x[0][0] ^ 0x80000000u, x[1][1] = x[0][1] ^ 0x80000000u;
    asm volatile("" : "+v"(x[0][0]), "+v"(x[0][1]), "+v"(x[1][0]), "+v"(x[1][1]), "+v"(fs)::"memory");
    sta4(w1, wv[0]);
    sta4(w1 + 1024, wv[1]);
    sta4(w0, wv[2]);
    sta4(w0 + 1024, wv[3]);
#pragma nounroll
    for (int jb = 0;; jb = 4) {
      u32x2 fw[4][2];
#pragma unroll
      for (int h = 0; h < 2; ++h) {
        fw[0][h] = lds_tr(wa + q * 256 + l15 * 8 + h * 128);
        fw[1][h] = lds_tr(wa + q * 256 + l15 * 8 + h * 128 + 1024);
        fw[2][h] = lds_tr(wb + (3 - q) * 256 + l15 * 8 + h * 128);
        fw[3][h] = lds_tr(wb + (3 - q) * 256 + l15 * 8 + h * 128 + 1024);
      }
      row_head(x, m16, m1, tw, !two);
#pragma unroll
      for (int i = 0; i < 8; ++i)
        fw[2 + (i >> 2)][(i >> 1) & 1][i & 1] =
            (uint32_t)__builtin_amdgcn_update_dpp(0, (int)(uint32_t)fw[2 + (i >> 2)][(i >> 1) & 1][i & 1], 0x140, 0xF, 0xF, true);
      const uint32_t s0 = __builtin_amdgcn_readlane(wsp, jb), s1 = __builtin_amdgcn_readlane(wsp, jb + 1);
#pragma unroll
      for (int t = 0; t < 2; ++t) {
        cf u[4];
        lds_cf2(smem + L_X + 6144 + t * 2048 + lane * 16, u[0], u[1]), lds_cf2(smem + L_X + 7168 + t * 2048 + lane * 16, u[2], u[3]);
        f32x4 ar = mfma_h(m16.p, x[0][t], Z4), ai = mfma_h(m16.q, x[0][t], Z4), br = mfma_h(m16.p, x[1][t], Z4), bi = mfma_h(m16.q, x[1][t], Z4);
        auto half = [](uint32_t w, int hi) { return u2f(hi ? w & 0xFFFF0000u : w << 16); };
        cf lo[4];
#pragma unroll
        for (int r = 0; r < 4; ++r) {
          const int rr = t ? 3 - r : r;
          lo[r] = fin(cf{half(fw[2 * t][rr >> 1][rr & 1], 0), half(fw[2 * t + 1][rr >> 1][rr & 1], 0)});
        }
        prio<0>(!two);
#pragma unroll
        for (int r = 0; r < 4; ++r) {
          const int rr = t ? r : 3 - r;
          const cf hi = cf{half(fw[2 - 2 * t][rr >> 1][rr & 1], 1), half(fw[3 - 2 * t][rr >> 1][rr & 1], 1)};
          cf fa = t ? hi : lo[r], fb = t ? lo[r] : hi;
          fb.y = -fb.y;
          if (t == 0 && r == 0 && lane == 0) {
            fa = (fa + cf{half(s0, 0), -half(s0, 1)}) * 0.5f;
            fb = (fb + cf{half(s1, 0), half(s1, 1)}) * 0.5f;
          }
          const cf tb = cf{br[r] * fs, bi[r] * fs};
          const cf E = cf{__builtin_fmaf(ar[r], fs, tb.x), __builtin_fmaf(ai[r], fs, tb.y)};
          const cf D = cf{__builtin_fmaf(ar[r], fs, -tb.x), __builtin_fmaf(ai[r], fs, -tb.y)};
          const cf X1 = cmad(u[r], D, E), X2 = cf{__builtin_fmaf(2.f, E.x, -X1.x), __builtin_fmaf(2.f, E.y, -X1.y)};
          const cf Yb = cmul(X2, fb), S = cmad(X1, fa, Yb);
          const cf Df = cf{__builtin_fmaf(-2.f, Yb.x, S.x), __builtin_fmaf(-2.f, Yb.y, S.y)};
          const cf O1 = fin(cmad(cf{u[r].x, -u[r].y}, Df, S));
          const cf O2 = fin(cf{__builtin_fmaf(2.f, S.x, -O1.x), __builtin_fmaf(-2.f, S.y, O1.y)});
          ar[r] = O1.x, ai[r] = O1.y, br[r] = O2.x, bi[r] = O2.y;
        }
        prio<1>(!two);
#pragma unroll
        for (int r = 0; r < 4; ++r) x[0][t][r] = pk_h(ar[r], -ai[r]), x[1][t][r] = pk_h(br[r], bi[r]);
      }
      if (!two) break;
      if (jb) {
        x[0][0] = ldsa(row + lane * 16), x[0][1] = ldsa(row + 1024 + lane * 16);
        break;
      }
      sta4(row + lane * 16, x[0][0]), sta4(row + 1024 + lane * 16, x[0][1]);
      rows();
      x[0][0] = x[1][0] ^ 0x80000000u, x[0][1] = x[1][1] ^ 0x80000000u;
      wa = ldsb + L_WS, wb = ldsb + L_WS + 2048;
    }
    row_head(x, m16, m1, tw, !two);
    const uint32_t oa = row + l15 * 128 + (q ^ (l15 & 7)) * 16;
#pragma unroll
    for (int i = 0; i < 4; ++i) {
      const f32x4 re = mfma_h(x[i >> 1][i & 1], m16.p, Z4), im = mfma_h(x[i >> 1][i & 1], m16.q, Z4);
      sta4((oa ^ ((i & 1) * 64)) + (i >> 1) * 16384, u32x4{pk_h(re[0], im[0]), pk_h(re[1], im[1]), pk_h(re[2], im[2]), pk_h(re[3], im[3])});
    }
    m8.p = lds4(smem + L_X + 12288 + lane * 16), m8.q = lds4(smem + L_X + 13312 + lane * 16);
    fsync(2);
    u32x4 yi[4];
#pragma unroll
    for (int e = 0; e < 4; ++e) yi[e] = lds4(smem + L_Z + (4 * q + e) * 2048 + f * 256 + (l15 ^ ((2 * f + (l15 >> 3)) & 7)) * 16);
    asm volatile("" : "+v"(yi[0]), "+v"(yi[1]), "+v"(yi[2]), "+v"(yi[3]));
    u32x4 o[4];
#pragma unroll
    for (int c = 0; c < 4; ++c) {
      const u32x4 y = u32x4{yi[0][c], yi[1][c], yi[2][c], yi[3][c]};
      const f32x4 re = mfma_h(m8.p, y, Z4), im = mfma_h(m8.q, y, Z4);
#pragma unroll
      for (int r = 0; r < 4; ++r) o[r][c] = pk_h(re[r], im[r]);
    }
    const int zc = (int)(WS_ZC - WS_ZB) + q * 4096 + f * 256 + l15 * 16;
    asm volatile("" : "+v"(o[0]), "+v"(o[1]), "+v"(o[2]), "+v"(o[3]));
    lds_wait<SW_HD, 1>(U, 4);
#pragma unroll
    for (int i = 0; i < 4; ++i)
      __builtin_amdgcn_raw_buffer_store_b128(o[i], rzb, zc + ((i & 1) ? (bid ? 256 - bid : 128) : bid) * 16384 + (i >> 1) * 2048, 0, 16);
    __builtin_amdgcn_s_setprio(0);
  } else {
    if (f >= 4) return true;
    if (f == 1) {
      lds_wait<SW_PUB, 4>(U, 4);
      lds_wait<SW_F1, 4>(U, 1);
      __builtin_amdgcn_s_sleep(10);
      poll_count<SW_G1>(ws, 4 * T.tm, 16, U);
    }
  }
  const int N = T.late ? 32 - NX : NX;
  if (T.late && f != 1) return false;
  bars(2);
  wait_vm<0>();
  bar();
  lane = lane_id();
  uint32_t vo = (32 * f + (lane >> 3)) * 16384 + (li & 7) * 2048 + (li >> 3) * 128 + ((lane & 7) ^ (lane >> 4)) * 16;
  uint32_t vp = (vo ^ 64) + 131072 - 1024;
  asm volatile("" : "+v"(vo), "+v"(vp));
  u32x4 pw;
  auto scales = [&]() {
    uint32_t mw = pw[0] > pw[1] ? pw[0] : pw[1], m2 = pw[2] > pw[3] ? pw[2] : pw[3];
    mw = wave_max_u32(mw > m2 ? mw : m2);
#pragma unroll
    for (int i = 0; i < 4; ++i) {
      const float sc = __builtin_amdgcn_ldexpf(1.0f, (int)(uint32_t)pw[i] - (int)mw);
      pw[i] = pk_h(sc, sc);
    }
    if (lane == 32) pw[0] = ex + mw;
    if (lane < 33) sta4(ldsb + L_RS + lane * 16, pw);
    wait_lgkm0();
    lds_signal<SW_RS>(U);
  };
  if (f == 1) {
    bars(T.late ? 3 : 1);
    {
      uint32_t c;
      asm volatile("s_mov_b64 exec, 1\n\tglobal_atomic_add %0, %1, %1, %2 sc0\n\ts_mov_b64 exec, -1\n\ts_barrier\n\ts_waitcnt vmcnt(0)"
                   : "=&v"(c)
                   : "v"(0), "s"(ws + WS_HF + (4 * T.tm + 2) * 128)
                   : "memory");
      if (__builtin_amdgcn_readfirstlane(c) < 32u) poll_count<SW_FR>(ws, 4 * T.tm + 2, 32, U);
    }
    if (T.late) {
      bars(N - 10);
      poll_count<SW_PX, true>(ws, 64 + li, 4, U);
      return false;
    }
    bars(N - 7);
  } else if (f == 0) {
    if (lane == 0) st_dev((uint32_t*)(ws + WS_P2) + bid, 256u | pay);
    bars(N - 7);
    const __amdgpu_buffer_rsrc_t rp = __builtin_amdgcn_make_buffer_rsrc((void*)(ws + WS_P2), 0, 1024, 0x00020000);
    pw = __builtin_amdgcn_raw_buffer_load_b128(rp, lane * 16, 0, 16);
    bars(2);
#pragma nounroll
    for (unsigned spins = 0; spins < SPIN_MAX; ++spins) {
      if (__builtin_amdgcn_ballot_w64((pw[0] & pw[1] & pw[2] & pw[3] & 256u) == 0) == 0) break;
      __builtin_amdgcn_s_sleep(8);
      pw = __builtin_amdgcn_raw_buffer_load_b128(rp, lane * 16, 0, 16);
      asm volatile("" ::: "memory");
    }
    lds_signal<SW_P2>(U);
    asm volatile("" : "+v"(pw));
  } else {
    bars(N - 5);
  }
  {
    constexpr uint32_t img = 256 + ((NX - 3) & 3) * G0_STAGE;
    if (f) lds_wait<SW_P2, 1>(U, 1);
    gdev2(ws + WS_ZC, vo, vp, img + f * 4096);
    gdev2(ws + WS_ZC + 262144, vo, vp, img + f * 4096 + 2048);
    if (f == 0) scales();
    lane = lane_id();
    l15 = lane & 15;
    q = lane >> 4;
    uint32_t xc = img + (l15 + 64 * q) * 128 + (f ^ (l15 >> 1)) * 16;
    uint32_t xr = img + f * 4096 + q * 128 + ((l15 >> 1) ^ (q >> 1)) * 16 + (l15 & 1) * 8;
    int oo = (32 * f + q) * 65536 + (li & 7) * 8192 + (li >> 3) * 256 + l15 * 16;
    lds_cf2(smem + L_TB + 2048 + lane * 16, tw[0][0], tw[0][1]), lds_cf2(smem + L_TB + 3072 + lane * 16, tw[0][2], tw[0][3]);
    asm volatile("" : "+v"(xc), "+v"(xr), "+v"(oo), "+v"(tw[0][0]), "+v"(tw[0][1]), "+v"(tw[0][2]), "+v"(tw[0][3]));
    lds_wait<SW_RS, 1>(U, 2);
    u32x4 wv[4], rs;
#pragma unroll
    for (int e = 0; e < 4; ++e) {
      const int k = l15 + 64 * q + 16 * e;
      rs[e] = *(lds_u32*)(size_t)(ldsb + L_RS + ((q < 2 ? k : -k) & 127) * 4);
    }
    const int sh = __builtin_amdgcn_readfirstlane((int)*(lds_u32*)(size_t)(ldsb + L_RS + 512)) - 511;
    float sc = __builtin_amdgcn_ldexpf(1.0f, sh);
    asm volatile("" : "+v"(rs), "+v"(sc));
    wait_vm<0>();
    fsync(3);
#pragma unroll
    for (int e = 0; e < 4; ++e) wv[e] = ldsa(xc + e * 2048);
    f32x4 dr[4], di[4];
#pragma unroll
    for (int c = 0; c < 4; ++c) {
      const u32x4 x = u32x4{pk_mul_h(wv[0][c], rs[0]), pk_mul_h(wv[1][c], rs[1]), pk_mul_h(wv[2][c], rs[2]), pk_mul_h(wv[3][c], rs[3])};
      dr[c] = mfma_h(x, m16.p, Z4);
      di[c] = mfma_h(x, m16.q, Z4);
    }
#pragma unroll
    for (int c = 0; c < 4; ++c) {
      u32x4 a;
#pragma unroll
      for (int r = 0; r < 4; ++r) {
        const cf v = fin(cmul(cf{dr[c][r], di[c][r]}, tw[0][r]));
        a[r] = pk_h(v.x, v.y);
      }
      dr[c] = mfma_h(m16.p, a, Z4);
      di[c] = mfma_h(m16.q, a, Z4);
    }
    auto put = [&](auto scale) {
#pragma unroll
      for (int r = 0; r < 4; ++r) {
        u32x4 y;
#pragma unroll
        for (int c = 0; c < 4; ++c) y[c] = pk_bf(scale(dr[c][r]), scale(-di[c][r]));
        sta4(xc + r * 2048, y);
      }
    };
    if (__builtin_expect(sh >= -126 && sh <= 127, 1)) put([&](float x) { return x * sc; });
    else put([&](float x) { return __builtin_amdgcn_ldexpf(x, sh); });
    fsync(4);
    typedef __attribute__((address_space(3))) u32x2 lds_u32x2;
    u32x2 z[8];
#pragma unroll
    for (int i = 0; i < 8; ++i) z[i] = *(lds_u32x2*)(size_t)((xr ^ ((i & 3) * 32)) + i * 512);
    const __amdgpu_buffer_rsrc_t ro = __builtin_amdgcn_make_buffer_rsrc((void*)A.out, 0, 16 << 20, 0x00020000);
    lds_wait<SW_PL, 2>(U, 4);
#pragma unroll
    for (int i = 0; i < 8; ++i) {
      const uint32_t p0 = z[i][0], p1 = z[i][1];
      __builtin_amdgcn_raw_buffer_store_b128(u32x4{p0 << 16, p0 & 0xFFFF0000u, p1 << 16, p1 & 0xFFFF0000u}, ro, oo + i * 262144, 0, 2);
    }
    if (f == 0 && lane_id() < 2) st_dev((u64*)(ws + WS_F1) + bid + lane_id() * 128, 0ull);
  }
  return false;
}

__device__ __forceinline__ void publish(char* ws, int idx, int lane) {
  wait_vm<0>();
  if (lane == 0) __hip_atomic_fetch_add((uint32_t*)(ws + WS_HF + idx * 128), 1u, __ATOMIC_RELAXED, __HIP_MEMORY_SCOPE_AGENT);
}
__device__ __forceinline__ void loader_role(const Args& A, char* smem, int wave, int lane) {
  char* const ws = A.ws;
  const Tile T = tile_map();
  const int tm = T.tm, tn = T.tn;
  lds_u32* const sw = (lds_u32*)(smem + L_SYNC);
  const uint32_t U = (uint32_t)(size_t)(sw + 1);
  const uint32_t ldsb = lds_base(smem), lds0 = ldsb + wave * 4096;
  const int prow = lane >> 3;
  {
    __builtin_amdgcn_s_setprio(0);
    const uint32_t va = (tm * 128 + wave * 32 + prow) * 2048 + ((lane & 7) ^ (prow & 3)) * 16;
    const uint32_t vb = (tn * 64 + wave * 16 + prow) * 2048 + ((lane & 7) ^ prow) * 16;
    if (T.late) {
      bar();
      const int wc = (bid_x() >> 3) & 15;
      if ((wc & 7) != 3) {
#pragma nounroll
        for (int i = 2 + wave; i < 7; i += 4) warm_r(A, smem, lane, wc, i);
      }
      stage0(A, smem, wave, lane, 0);
    }
    stage0(A, smem, wave, lane, 1);
    stage0(A, smem, wave, lane, 2);
#pragma nounroll
    for (int k = 3; k < 16; ++k) {
      const uint32_t dst = lds0 + (k & 3) * G0_STAGE;
      const int ko = ((k + k_rot()) & 15) * 64;
      asm volatile(
          "s_mov_b32 m0, %9\n\ts_waitcnt vmcnt(8)\n\ts_barrier\n\t"
          "global_load_lds_dwordx4 %0, %6\n\tglobal_load_lds_dwordx4 %1, %6 offset:1024\n\t"
          "global_load_lds_dwordx4 %2, %6 offset:2048\n\tglobal_load_lds_dwordx4 %3, %6 offset:3072\n\t"
          "s_mov_b32 m0, %10\n\ts_nop 0\n\t"
          "global_load_lds_dwordx4 %4, %7\n\tglobal_load_lds_dwordx4 %5, %7 offset:1024\n\t"
          "global_load_lds_dwordx4 %4, %8 offset:2048\n\tglobal_load_lds_dwordx4 %5, %8 offset:3072"
          ::"v"(va), "v"(va + 15360), "v"((va ^ 64) + 2 * 15360), "v"((va ^ 64) + 3 * 15360), "v"(vb), "v"(vb + 15360),
            "s"(A.xB + ko), "s"(A.Bre + ko), "s"(A.Bim + ko - 1024), "s"(dst), "s"(dst + 16384)
          : "memory");
    }
    wait_vm<8>();
    bar();
    wait_vm<0>();
    lds_signal<SW_LD>(U);
  }
  __builtin_amdgcn_s_setprio(2);
  {
    const int nseg = 2 * tm - 8 * wave;
    cf G = cf{0.f, 0.f};
    lds_wait<SW_F1, 2>(U, 1);
    if (nseg > 0) {
      const __amdgpu_buffer_rsrc_t ra = __builtin_amdgcn_make_buffer_rsrc((void*)(ws + WS_AGG), 0, 512 << 10, 0x00020000);
      int o[8];
#pragma unroll
      for (int k = 0; k < 8; ++k) {
        o[k] = ((nseg - 1 - k) * 1024 + tn * 64 + lane) * 16;
        asm volatile("" : "+v"(o[k]));
      }
      u32x4 u[8];
      cf l64 = reinterpret_cast<const cf*>(smem + L_PART)[192 + lane], l128 = reinterpret_cast<const cf*>(smem + L_PART)[384 + lane];
      cf lw = reinterpret_cast<const cf*>(smem + L_GRP)[wave * 64 + lane];
      asm volatile("" : "+v"(l64), "+v"(l128), "+v"(lw));
      lds_wait<SW_PUB, 1>(U, 4);
      __builtin_amdgcn_s_sleep(10);
#pragma nounroll
      for (unsigned spins = 0; spins < SPIN_MAX; ++spins) {
        uint32_t n = 0;
#pragma unroll
        for (int k = 0; k < 8; ++k) u[k] = __builtin_amdgcn_raw_buffer_load_b128(ra, o[k], 0, 16);
#pragma unroll
        for (int k = 0; k < 8; ++k) n += u[k][1] + u[k][3];
        if (__builtin_amdgcn_ballot_w64(n != 2u * (nseg < 8 ? nseg : 8)) == 0) break;
        asm volatile("" ::: "memory");
      }
#pragma unroll
      for (int k = 6; k >= 0; k -= 2)
        G = cmad(G, l128, cmad(cf{u2f(u[k + 1][0]), u2f(u[k + 1][2])}, l64, cf{u2f(u[k][0]), u2f(u[k][2])}));
      G = cmul(G, lw);
    }
    reinterpret_cast<cf*>(smem + L_GRP)[wave * 64 + lane] = G;
    wait_lgkm0();
    lds_signal<SW_GRP>(U);
  }
  if (!T.late) {
    lds_wait<SW_GRP, 2>(U, 4);
    lds_wait<SW_G0E, 2>(U, 4);
    stage_c(A, wave, lane, 0);
    lds_wait<SW_HS, 2>(U, 4);
    stage_c(A, wave, lane, 1);
    stage_c(A, wave, lane, 2);
  }
  lds_wait<SW_G1, 1>(U, 1);
  __builtin_amdgcn_s_setprio(0);
  const int N = T.late ? 32 - NX : NX;
  const char* ab = ws + WS_HS + (size_t)tm * (64 * 8192);
  const uint32_t vh = (wave >> 1) * 262144 + (wave & 1) * 4096 + lane * 16, vc = g1_vc(wave, lane);
#pragma nounroll
  for (int k = T.late ? 2 : 0; k < 3; ++k) {
    const char* pa = ab + g1_kt(T.late, k) * 8192;
    glds2_dev(pa, vh, lds0 + (k + 1) * G0_STAGE);
    glds2_dev(pa + 2048, vh, lds0 + (k + 1) * G0_STAGE + 2048);
  }
  wait_vm<4>();
#pragma nounroll
  for (int k = 3; k < N; ++k) {
    const uint32_t dst = lds0 + ((k + 1) & 3) * G0_STAGE;
    const int kt = g1_kt(T.late, k);
    asm volatile(
        "s_mov_b32 m0, %5\n\ts_waitcnt vmcnt(8)\n\ts_barrier\n\t"
        "global_load_lds_dwordx4 %2, %4 sc1\n\tglobal_load_lds_dwordx4 %2, %4 offset:1024 sc1\n\t"
        "global_load_lds_dwordx4 %2, %4 offset:2048 sc1\n\tglobal_load_lds_dwordx4 %2, %4 offset:3072 sc1\n\t"
        "s_mov_b32 m0, %6\n\ts_nop 0\n\t"
        "global_load_lds_dwordx4 %0, %3\n\tglobal_load_lds_dwordx4 %1, %3 offset:1024\n\t"
        "global_load_lds_dwordx4 %0, %3 offset:2048\n\tglobal_load_lds_dwordx4 %1, %3 offset:3072"
        ::"v"(vc), "v"(vc + 65536 - 1024), "v"(vh), "s"(((kt & 1) ? A.Cim : A.Cre) + (kt >> 1) * 64), "s"(ab + kt * 8192), "s"(dst),
          "s"(dst + 16384)
        : "memory");
  }
  wait_vm<8>();
  bar();
  wait_vm<0>();
  lds_signal<SW_LD>(U);
  if (T.late) {
    const char* ps = ws + WS_PS + (size_t)(bid_x() & 127) * 32768 + wave * 8192;
    lds_wait<SW_PX, 1>(U, 1);
#pragma nounroll
    for (int i = 0; i < 4; ++i) glds2_dev(ps + i * 2048, lane * 16, ldsb + ((29 - NX) & 3) * G0_STAGE + wave * 8192 + i * 2048);
    wait_vm<0>();
    lds_signal<SW_PL>(U);
  }
}

#define W(n) "s_waitcnt lgkmcnt(" #n ")\n\t"
#define M(c, a, b) "v_mfma_f32_16x16x32_bf16 v[" #c ":" #c "+3], v[" #a ":" #a "+3], v[" #b ":" #b "+3], v[" #c ":" #c "+3]\n\t"
#define RB(d, p, o) "ds_read_b128 v[" #d ":" #d "+3], v" #p " offset:" #o "\n\t"
#define RT(d, p, o) "ds_read_b64_tr_b16 v[" #d ":" #d "+1], v" #p " offset:" #o "\n\t"
#define AD(p) "v_add_u32 v" #p ", %[adv], v" #p "\n\t"
#define SA "s_add_i32 %[k], %[k], 1\n\ts_and_b32 %[adv], %[k], 3\n\t"
#define SB "s_cmp_eq_u32 %[adv], 0\n\ts_cselect_b32 %[adv], %[m3], 0x8000\n\t"
#define F(d, p, l) "v_fmac_f32 v[" #d "], v[" #p "], v" #l "\n\t"
#define XA(d, p) F(d, p + 4, 122) F(d + 4, p + 4, 104) F(d + 8, p + 12, 123) F(d + 12, p + 12, 106)
#define XB(d, p) F(d, p, 104) F(d + 4, p, 105) F(d + 8, p + 8, 106) F(d + 12, p + 8, 107)
#define LA0 "v_mbcnt_lo_u32_b32 v108, -1, 0\n\tv_mbcnt_hi_u32_b32 v108, -1, v108\n\t"
#define LA1 "v_and_b32 v108, 15, v108\n\tv_lshl_add_u32 v108, v108, 3, %[pp]\n\t"
#define LAM "ds_read_b64 v[104:105], v108 offset:%[lo]\n\tds_read_b64 v[106:107], v108 offset:%[lo]+128\n\ts_setprio 3\n\t"
#define L16 "ds_read2_b64 v[64:67], v108 offset0:64 offset1:80\n\tds_read2_b64 v[68:71], v108 offset0:128 offset1:144\n\t"
#define NEG "v_xor_b32 v122, 0x80000000, v105\n\tv_xor_b32 v123, 0x80000000, v107\n\t"
#define SIG "v_mov_b32 v109, 4\n\ts_mov_b64 exec, 1\n\tds_add_u32 v109, v109 offset:%[e]\n\ts_mov_b64 exec, -1\n\t"
#define STEP0(C0, CN, H4, H9, H19, H30) \
  W(7) M(0, 64, C0) RB(72, 120, 512) W(7) M(0, 68, C0 + 4) RB(76, 121, 512) \
  W(7) M(4, 64, 80) W(6) M(4, 68, 84) \
  W(5) M(8, 64, 88) SA H4 W(4) M(8, 68, 92) SB \
  W(3) M(12, 64, 96) RB(64, 120, 1024) W(3) M(12, 68, 100) RB(68, 121, 1024) \
  W(3) M(16, 72, C0) W(2) M(16, 76, C0 + 4) AD(122) H9 \
  M(20, 72, 80) RB(CN, 122, 0) M(20, 76, 84) AD(123) \
  M(24, 72, 88) RB(CN + 4, 123, 0) M(24, 76, 92) \
  M(28, 72, 96) RB(72, 120, 1536) M(28, 76, 100) RB(76, 121, 1536) \
  W(5) M(32, 64, C0) W(4) M(32, 68, C0 + 4) \
  M(36, 64, 80) M(36, 68, 84) H19 \
  M(40, 64, 88) AD(120) M(40, 68, 92) AD(121) \
  M(44, 64, 96) RB(64, 120, 0) M(44, 68, 100) RB(68, 121, 0) \
  W(3) M(52, 72, 80) RB(80, 122, 2048) W(3) M(52, 76, 84) RB(84, 123, 2048) \
  M(56, 72, 88) RB(88, 122, 4096) M(56, 76, 92) RB(92, 123, 4096) \
  M(60, 72, 96) RB(96, 122, 6144) M(60, 76, 100) RB(100, 123, 6144) \
  M(48, 72, C0) H30 M(48, 76, C0 + 4)
#define HEAD0(C0) \
  RB(C0, 122, 0) RB(C0 + 4, 123, 0) RB(64, 120, 0) RB(68, 121, 0) RB(80, 122, 2048) RB(84, 123, 2048) RB(88, 122, 4096) RB(92, 123, 4096) RB(96, 122, 6144) RB(100, 123, 6144)
#define STEP1(C0, CN, H4, H9, H19, H30) \
  W(8) M(0, 64, C0) RT(72, 120, 2048) W(6) M(0, 68, C0 + 4) RT(74, 121, 2048) \
  M(4, 64, 80) RT(76, 120, 2560) W(7) M(4, 68, 84) RT(78, 121, 2560) \
  W(7) M(8, 64, 88) SA H4 W(6) M(8, 68, 92) SB \
  W(5) M(12, 64, 96) RT(64, 120, 4096) W(5) M(12, 68, 100) RT(66, 121, 4096) \
  W(4) M(16, 72, C0) RT(68, 120, 4608) W(3) M(16, 76, C0 + 4) RT(70, 121, 4608) \
  M(20, 72, 80) AD(122) H9 M(20, 76, 84) RB(CN, 122, 0) \
  M(24, 72, 88) AD(123) M(24, 76, 92) RB(CN + 4, 123, 0) \
  M(28, 72, 96) RT(72, 120, 6144) M(28, 76, 100) RT(74, 121, 6144) \
  W(6) M(32, 64, C0) RT(76, 120, 6656) W(5) M(32, 68, C0 + 4) RT(78, 121, 6656) \
  M(36, 64, 80) M(36, 68, 84) H19 \
  M(40, 64, 88) AD(120) M(40, 68, 92) AD(121) \
  M(44, 64, 96) RT(64, 120, 0) M(44, 68, 100) RT(66, 121, 0) \
  W(4) M(52, 72, 80) RT(68, 120, 512) RB(80, 122, 2048) W(4) M(52, 76, 84) RT(70, 121, 512) RB(84, 123, 2048) \
  M(56, 72, 88) RB(88, 122, 4096) M(56, 76, 92) RB(92, 123, 4096) \
  M(60, 72, 96) RB(96, 122, 6144) M(60, 76, 100) RB(100, 123, 6144) \
  M(48, 72, C0) H30 M(48, 76, C0 + 4)
#define HEAD1(C0) \
  RB(C0, 122, 0) RB(C0 + 4, 123, 0) RT(64, 120, 0) RT(66, 121, 0) RT(68, 120, 512) RB(80, 122, 2048) RT(70, 121, 512) RB(84, 123, 2048) RB(88, 122, 4096) RB(92, 123, 4096) RB(96, 122, 6144) RB(100, 123, 6144)
#define ROW1(C0) \
  W(8) M(0, 64, C0) RT(72, 120, 2048) W(6) M(0, 68, C0 + 4) RT(74, 121, 2048) \
  M(4, 64, 80) RT(76, 120, 2560) W(7) M(4, 68, 84) RT(78, 121, 2560) \
  W(7) M(8, 64, 88) W(6) M(8, 68, 92) \
  W(5) M(12, 64, 96) W(4) M(12, 68, 100) \
  W(0)
#define TAIL0 \
  W(7) M(0, 64, 112) RB(72, 120, 512) LA0 W(7) M(0, 68, 116) RB(76, 121, 512) LA1 \
  W(7) M(4, 64, 80) LAM W(8) M(4, 68, 84) \
  W(7) M(8, 64, 88) W(6) M(8, 68, 92) \
  W(5) M(12, 64, 96) RB(64, 120, 1024) W(5) M(12, 68, 100) RB(68, 121, 1024) \
  W(5) M(16, 72, 112) W(2) NEG M(16, 76, 116) \
  M(20, 72, 80) XA(1, 0) M(20, 76, 84) XB(1, 0) \
  M(24, 72, 88) XA(2, 1) M(24, 76, 92) XB(2, 1) \
  M(28, 72, 96) RB(72, 120, 1536) XA(3, 2) M(28, 76, 100) RB(76, 121, 1536) XB(3, 2) \
  W(3) M(32, 64, 112) W(2) M(32, 68, 116) \
  M(36, 64, 80) XA(16, 3) M(36, 68, 84) XB(16, 3) \
  M(40, 64, 88) XA(17, 16) M(40, 68, 92) XB(17, 16) \
  M(44, 64, 96) XA(18, 17) M(44, 68, 100) XB(18, 17) \
  W(1) M(48, 72, 112) XA(19, 18) W(0) M(48, 76, 116) SIG XB(19, 18) \
  M(52, 72, 80) L16 XA(32, 19) M(52, 76, 84) XB(32, 19) \
  M(56, 72, 88) XA(33, 32) M(56, 76, 92) XB(33, 32) \
  M(60, 72, 96) XA(34, 33) M(60, 76, 100) XB(34, 33) \
  XA(35, 34) XB(35, 34) "s_nop 3\n\t" XA(48, 35) XB(48, 35) \
  XA(49, 48) XB(49, 48) XA(50, 49) XB(50, 49) \
  XA(51, 50) XB(51, 50) W(0)
#define V2(op, d, a, b) op " v[" #d "], " a ", v[" #b "]\n\t"
#define CV(d, a) "v_cvt_pk_bf16_f32 v[" #d "], v[" #a "], v[" #a "+4]\n\t"
#define ST(d, inc, pol) "buffer_store_dwordx4 v[" #d ":" #d "+3], v123, %[rs], %[sf] offen " pol "\n\ts_add_u32 %[sf], %[sf], " #inc "\n\t"
#define C4(d, a) CV(d, a) CV(d + 1, a + 8) CV(d + 2, a + 1) CV(d + 3, a + 9)
#define SP(d) ST(d, 1024, "sc1")
#define UN(d, p) V2("v_lshlrev_b32", d, "16", p) V2("v_and_b32", d + 1, "0xffff0000", p) V2("v_lshlrev_b32", d + 2, "16", p + 1) V2("v_and_b32", d + 3, "0xffff0000", p + 1)
#define A1(d, a) "v_add_f32 v[" #d "], v[" #a "], v[" #d "]\n\t"
#define AS(d, a, inc) A1(d, a) A1(d + 1, a + 4) A1(d + 2, a + 8) A1(d + 3, a + 12) ST(d, inc, "nt")
#define LE1 "v_lshlrev_b32 v123, 4, v108\n\t"
#define LL1 "v_lshl_add_u32 v122, v108, 4, %[pp]\n\tv_and_b32 v123, 15, v108\n\t"
#define LL2 "v_lshrrev_b32 v108, 4, v108\n\tv_lshlrev_b32 v108, 15, v108\n\tv_lshl_add_u32 v123, v123, 4, v108\n\t"
#define PLW "v_mov_b32 v104, 0\n6:\n\tds_read_b32 v105, v104 offset:%[e]\n\t" W(0) "v_readfirstlane_b32 %[t], v105\n\ts_cmp_lt_u32 %[t], 16\n\ts_cbranch_scc1 6b\n\t"
#define TAIL1E \
  RT(64, 120, 4096) RT(66, 121, 4096) RT(68, 120, 4608) RT(70, 121, 4608) M(16, 72, 112) LA0 LE1 M(16, 76, 116) \
  M(20, 72, 80) M(20, 76, 84) \
  M(24, 72, 88) M(24, 76, 92) C4(104, 0) SP(104) \
  M(28, 72, 96) RT(72, 120, 6144) M(28, 76, 100) RT(74, 121, 6144) C4(108, 2) SP(108) \
  W(4) M(32, 64, 112) RT(76, 120, 6656) W(3) M(32, 68, 116) RT(78, 121, 6656) \
  M(36, 64, 80) M(36, 68, 84) \
  M(40, 64, 88) M(40, 68, 92) C4(0, 16) SP(0) \
  M(44, 64, 96) M(44, 68, 100) C4(4, 18) SP(4) \
  W(2) M(48, 72, 112) W(0) M(48, 76, 116) \
  M(52, 72, 80) M(52, 76, 84) \
  M(56, 72, 88) M(56, 76, 92) C4(8, 32) SP(8) \
  M(60, 72, 96) M(60, 76, 100) C4(12, 34) SP(12) \
  "s_nop 7\n\t" C4(16, 48) C4(20, 50) \
  SP(16) SP(20)
#define TAIL1L \
  RT(64, 120, 4096) RT(66, 121, 4096) RT(68, 120, 4608) RT(70, 121, 4608) M(16, 72, 112) LA0 LL1 M(16, 76, 116) LL2 \
  M(20, 72, 80) PLW RB(104, 122, 0) M(20, 76, 84) \
  M(24, 72, 88) W(0) UN(108, 104) M(24, 76, 92) AS(108, 0, 8192) \
  M(28, 72, 96) RT(72, 120, 6144) RB(108, 122, 1024) UN(104, 106) M(28, 76, 100) RT(74, 121, 6144) AS(104, 1, 8192) \
  M(32, 64, 112) RT(76, 120, 6656) W(2) UN(104, 108) M(32, 68, 116) RT(78, 121, 6656) AS(104, 2, 8192) \
  M(36, 64, 80) RB(104, 122, 2048) UN(108, 110) M(36, 68, 84) AS(108, 3, 106496) \
  M(40, 64, 88) RB(108, 122, 3072) M(40, 68, 92) W(1) UN(0, 104) \
  M(44, 64, 96) AS(0, 16, 8192) M(44, 68, 100) UN(4, 106) \
  M(48, 72, 112) AS(4, 17, 8192) RB(104, 122, 4096) M(48, 76, 116) W(1) UN(8, 108) \
  M(52, 72, 80) AS(8, 18, 8192) M(52, 76, 84) UN(12, 110) \
  M(56, 72, 88) AS(12, 19, 106496) RB(108, 122, 5120) M(56, 76, 92) W(1) UN(16, 104) \
  M(60, 72, 96) AS(16, 32, 8192) M(60, 76, 100) UN(20, 106) \
  AS(20, 33, 8192) RB(104, 122, 6144) W(1) UN(24, 108) AS(24, 34, 8192) UN(28, 110) \
  AS(28, 35, 106496) RB(108, 122, 7168) "s_nop 7\n\t" W(1) UN(32, 104) AS(32, 48, 8192) UN(36, 106) \
  AS(36, 49, 8192) W(0) UN(40, 108) AS(40, 50, 8192) UN(44, 110) \
  AS(44, 51, 106496)
#define ASK "v_mov_b32 v112, 0\n\tds_read_b32 v112, v112 offset:%[o]\n\t"
#define CHK "v_readfirstlane_b32 %[t], v112\n\ts_cmp_lt_u32 %[t], %[cnt]\n\t"
#define LOOP(X)                                                                                                           \
  "1:\n\t" STEP##X(104, 112, , , , "s_barrier\n\t") "5:\n\t"                                                                \
  STEP##X(112, 104, , , "s_cmp_lg_u32 %[k], %[k1]\n\t", "s_cbranch_scc0 2f\n\ts_barrier\n2:\n\t") "s_cbranch_scc1 1b\n\t" \
  STEP##X(104, 112, ASK, CHK "s_cbranch_scc0 4f\n3:\n\t" ASK W(0) CHK "s_cbranch_scc1 3b\n4:\n\t", , )
#define ACC \
  "+{v[0:3]}"(acc[0][0]), "+{v[4:7]}"(acc[0][1]), "+{v[8:11]}"(acc[0][2]), "+{v[12:15]}"(acc[0][3]), \
  "+{v[16:19]}"(acc[1][0]), "+{v[20:23]}"(acc[1][1]), "+{v[24:27]}"(acc[1][2]), "+{v[28:31]}"(acc[1][3]), \
  "+{v[32:35]}"(acc[2][0]), "+{v[36:39]}"(acc[2][1]), "+{v[40:43]}"(acc[2][2]), "+{v[44:47]}"(acc[2][3]), \
  "+{v[48:51]}"(acc[3][0]), "+{v[52:55]}"(acc[3][1]), "+{v[56:59]}"(acc[3][2]), "+{v[60:63]}"(acc[3][3]), \
  "+{v120}"(pa0), "+{v121}"(pa1), "+{v122}"(pb0), "+{v123}"(pb1)
#define CLOB \
  "scc", "memory", "v64", "v65", "v66", "v67", "v68", "v69", "v70", "v71", "v72", "v73", "v74", "v75", "v76", "v77", "v78", "v79", \
  "v80", "v81", "v82", "v83", "v84", "v85", "v86", "v87", "v88", "v89", "v90", "v91", "v92", "v93", "v94", "v95", "v96", "v97", \
  "v98", "v99", "v100", "v101", "v102", "v103", "v104", "v105", "v106", "v107", "v108", "v109", "v110", "v111", "v112", "v113", \
  "v114", "v115", "v116", "v117", "v118", "v119"
__device__ __forceinline__ void run0(f32x4 (&acc)[4][4], uint32_t pa0, uint32_t pb0, uint32_t pp, u32x4& lam, u32x4& l16, u32x4& l32) {
  uint32_t pa1 = pa0 ^ 64, pb1 = pb0 ^ 64, t;
  int k = 0, adv;
  asm volatile("" : ACC);
  bar_lds();
  asm volatile(HEAD0(104) LOOP(0) TAIL0
               : ACC, [k] "+s"(k), [adv] "=&s"(adv), [t] "=&s"(t)
               : [k1] "n"(14), [m3] "s"(-3 * G0_STAGE), [cnt] "n"(16), [o] "n"(SW_LD * 4), [e] "n"(SW_G0E * 4 - 4), [pp] "s"(pp), [lo] "n"(L_PRM - L_PART)
               : CLOB);
  asm volatile("" : "={v[104:107]}"(lam), "={v[64:67]}"(l16), "={v[68:71]}"(l32));
  __builtin_amdgcn_sched_barrier(0);
}
__device__ __forceinline__ void run1(f32x4 (&acc)[4][4], uint32_t pa0, uint32_t pb0, int n, uint32_t pp, u32x4 rs, uint32_t sf) {
  uint32_t pa1 = pa0 ^ 64, pb1 = pb0 ^ 64, t;
  int k = 1, adv;
  asm volatile("" : ACC);
  bar_lds();
#define REST \
  LOOP(1) ROW1(112) "s_cmp_lg_u32 %[pp], 0\n\ts_cbranch_scc1 7f\n\t" TAIL1E "s_branch 8f\n7:\n\t" TAIL1L "8:" \
  : ACC, [k] "+s"(k), [adv] "=&s"(adv), [t] "=&s"(t), [sf] "+s"(sf) \
  : [k1] "s"(n - 1), [m3] "s"(-3 * G0_STAGE), [cnt] "n"(32), [o] "n"(SW_LD * 4), [e] "n"(SW_PL * 4), [pp] "s"(pp), [rs] "s"(rs) \
  : CLOB
  if (NX & 1) asm volatile(HEAD1(112) "s_branch 5f\n\t" REST);
  else asm volatile(HEAD1(104) REST);
}
#undef W
#undef M

__device__ __forceinline__ void mfma_role(const Args& A, char* smem, int wave, int lane) {
  char* const ws = A.ws;
  const Tile T = tile_map();
  const int tm = T.tm, tn = T.tn;
  lds_u32* const sw = (lds_u32*)(smem + L_SYNC);
  const uint32_t U = (uint32_t)(size_t)(sw + 1);
  const int wr = wave >> 1, wc = wave & 1;
  int fr = lane & 15, fq = lane >> 4;
  {
    if (T.late) bar_lds();
  }
  if (wave == 3) {
    wait_vm<0>();
    const u16* raw = reinterpret_cast<const u16*>(smem + L_PRM + 512);
    uint32_t pr = raw[lane], pi = raw[64 + lane], pd = raw[128 + lane];
    asm volatile("" : "+v"(pr), "+v"(pi), "+v"(pd)::"memory");
    auto expf_ = [](float x) {
      const float t = x * 1.44269504088896341f;
      const float r = __builtin_fmaf(x, 1.44269504088896341f, -t) + x * 1.92596299112661746e-8f;
      const float e = __builtin_amdgcn_exp2f(t);
      return __builtin_fmaf(e * r, 0.693147180559945309f, e);
    };
    const float lr = u2f(pr << 16), li = u2f(pi << 16);
    const float dt = rbf(expf_(u2f(pd << 16)));
    const float zr = dt * lr, zi = dt * li;
    const float e = expf_(zr);
    const float rh = zi * 0.159154936671257019f;
    const float rf = (rh - __builtin_rintf(rh)) + __builtin_fmaf(zi, 6.42063832653e-9f, __builtin_fmaf(zi, 0.159154936671257019f, -rh));
    const cf lam = cf{e * __builtin_amdgcn_cosf(rf), e * __builtin_amdgcn_sinf(rf)};
    const float nr = lam.x - 1.0f, ni = lam.y;
    const float den = 2.0f / (lr * lr + li * li);
    cf* prm = reinterpret_cast<cf*>(smem + L_PRM);
    prm[lane] = lam;
    prm[64 + lane] = cf{(nr * lr + ni * li) * den, (ni * lr - nr * li) * den};
    cf lp[10];
    lp[0] = cmul(lam, lam);
#pragma unroll
    for (int k = 1; k < 10; ++k) lp[k] = cmul(lp[k - 1], lp[k - 1]);
    cf* pt = reinterpret_cast<cf*>(smem + L_PART) + lane;
    pt[64] = lp[3], pt[128] = lp[4], pt[192] = lp[5], pt[256] = lp[0], pt[320] = lp[1], pt[384] = lp[6];
    pt = reinterpret_cast<cf*>(smem + L_GRP) + lane;
    pt[0] = cf{1.f, 0.f}, pt[64] = lp[8], pt[128] = lp[9], pt[192] = cmul(lp[8], lp[9]);
  }
  __builtin_amdgcn_s_setprio(1);
  {
    f32x4 acc[4][4];
#pragma unroll
    for (int m = 0; m < 4; ++m)
#pragma unroll
      for (int n = 0; n < 4; ++n) acc[m][n] = Z4;
    const cf* prm = reinterpret_cast<const cf*>(smem + L_PRM) + wc * 32;
    cf* part = reinterpret_cast<cf*>(smem + L_PART) + wc * 32;
    cf lam[2], l16[2], l32[2], s[2][16];
    const uint64_t ag = (uint64_t)(ws + WS_AGG) + ((2 * tm + wr) * 1024 + tn * 64 + wc * 32) * 16;
    u32x4 ra = u32x4{(uint32_t)ag, (uint32_t)(ag >> 32) & 0xFFFFu, 512u, 0x00020000u};
    {
      u32x4 t0, t1, t2;
      asm volatile("" : "+s"(ra));
      run0(acc, lds_base(smem) + (wr * 64 + (fr >> 2) * 16 + (fr & 3)) * 128 + (fq ^ (fr & 7)) * 16,
           lds_base(smem) + (128 + wc * 64 + fr) * 128 + (fq ^ (fr & 7)) * 16, lds_base(smem) + L_PART + wc * 256, t0, t1, t2);
#pragma unroll
      for (int jp = 0; jp < 2; ++jp) {
        lam[jp] = cf{u2f(t0[2 * jp]), u2f(t0[2 * jp + 1])};
        l16[jp] = cf{u2f(t1[2 * jp]), u2f(t1[2 * jp + 1])};
        l32[jp] = cf{u2f(t2[2 * jp]), u2f(t2[2 * jp + 1])};
      }
      lane = lane_id();
      fr = lane & 15;
      fq = lane >> 4;
#pragma unroll
      for (int i = 0; i < 32; ++i) s[i >> 4][i & 15] = cf{acc[(i >> 2) & 3][2 * (i >> 4)][i & 3], acc[(i >> 2) & 3][2 * (i >> 4) + 1][i & 3]};
    }
    const uint64_t m1 = 0xFFFF0000FFFF0000ull, m2 = 0xFFFFFFFF00000000ull;
    cf pe[2], a01[2], wg[2];
#pragma unroll
    for (int jp = 0; jp < 2; ++jp) {
      const u32x2 ex = __builtin_amdgcn_permlane16_swap(f2u(s[jp][15].x), f2u(s[jp][15].x), false, false);
      const u32x2 ey = __builtin_amdgcn_permlane16_swap(f2u(s[jp][15].y), f2u(s[jp][15].y), false, false);
      pe[jp] = cf{u2f(ex[0]), u2f(ey[0])};
      const cf ap = cmad(pe[jp], l16[jp], cf{u2f(ex[1]), u2f(ey[1])});
      const u32x2 hx = __builtin_amdgcn_permlane32_swap(f2u(ap.x), f2u(ap.x), false, false);
      const u32x2 hy = __builtin_amdgcn_permlane32_swap(f2u(ap.y), f2u(ap.y), false, false);
      a01[jp] = cf{u2f(hx[0]), u2f(hy[0])};
      wg[jp] = cmad(a01[jp], l32[jp], cf{u2f(hx[1]), u2f(hy[1])});
    }
    {
      const cf ws_ = sel_c(m1, wg[1], wg[0]);
      asm volatile("buffer_store_dwordx4 %0, %1, %2, 0 offen sc1\n\ts_nop 1"
                   ::"v"(u32x4{f2u(ws_.x), 1u, f2u(ws_.y), 1u}), "v"(lane * 16), "s"(ra) : "memory");
      if (wr == 0) {
        part[lane & 31] = ws_;
        wait_lgkm0();
      }
      lds_signal<SW_PUB>(U);
      if (wr) lds_wait<SW_PUB, 0>(U, 4);
    }
    __builtin_amdgcn_s_setprio(0);
    cf l2[2], l3[2], l4[2], c[2], pw[2];
#pragma unroll
    for (int jp = 0; jp < 2; ++jp) {
      const cf cof = prm[64 + jp * 16 + fr];
      l2[jp] = part[256 + jp * 16 + fr];
      l4[jp] = part[320 + jp * 16 + fr];
      const cf p1 = sel_c(m1, l16[jp], cf{1.f, 0.f});
      cf cc = sel_c(m1, pe[jp], cf{0.f, 0.f});
      cc = sel_c(m2, cmad(p1, a01[jp], cc), cc);
      cf p = sel_c(m2, cmul(p1, l32[jp]), p1);
      if (wr) {
        cc = cmad(p, part[jp * 16 + fr], cc);
        p = cmul(p, part[192 + jp * 16 + fr]);
      }
      pw[jp] = cmul(p, cof);
      c[jp] = cmul(cc, cof);
      l3[jp] = cmul(l2[jp], lam[jp]);
      asm volatile("" : "+v"(pw[jp]), "+v"(c[jp]), "+v"(l3[jp]), "+v"(l4[jp]));
#pragma unroll
      for (int i = 0; i < 16; ++i) {
        s[jp][i] = cmul(s[jp][i], cof);
        asm volatile("" : "+v"(s[jp][i]));
      }
    }
    const uint64_t hs = (uint64_t)(ws + WS_HS) + ((tm * 2 + wr) * 32 + tn * 2) * 8192;
    u32x4 rs = u32x4{(uint32_t)hs, (uint32_t)(hs >> 32) & 0xFFFFu, 16384u, 0x00020000u};
    uint32_t hb[2], ho[2], ga = lds_base(smem) + L_GRP + (wc * 32 + fr) * 8;
#pragma unroll
    for (int h = 0; h < 2; ++h) hb[h] = (fq * 2048 + (wc * 32 + fr) * 16) ^ (h * 64), ho[h] = hb[h] + lds_base(smem) + G0_STAGE + wr * 8192;
    asm volatile("" : "+s"(rs), "+v"(hb[0]), "+v"(hb[1]), "+v"(ho[0]), "+v"(ho[1]), "+v"(ga));
    lds_wait<SW_G0E, 1>(U, 4);
    __builtin_amdgcn_s_setprio(3);
    lds_wait<SW_GRP, 1>(U, 4);
    typedef __attribute__((address_space(3))) cf lds_cf;
    cf t[2];
#pragma unroll
    for (int jp = 0; jp < 2; ++jp) {
      const lds_cf* g = (const lds_cf*)(size_t)ga + jp * 16;
      t[jp] = cmad(pw[jp], (g[192] + g[128]) + (g[64] + g[0]), c[jp]);
    }
    asm volatile("" : "+v"(t[0]), "+v"(t[1]));
#pragma unroll
    for (int jh = 0; jh < 4; ++jh) {
      const int jp = jh >> 1, h = jh & 1;
#pragma unroll
      for (int k = 2 * h; k < 2 * h + 2; ++k) {
        s[jp][4 * k] = cmad(t[jp], lam[jp], s[jp][4 * k]);
        s[jp][4 * k + 1] = cmad(t[jp], l2[jp], s[jp][4 * k + 1]);
        s[jp][4 * k + 2] = cmad(t[jp], l3[jp], s[jp][4 * k + 2]);
        t[jp] = cmul(t[jp], l4[jp]);
        s[jp][4 * k + 3] = s[jp][4 * k + 3] + t[jp];
      }
      u32x4 re, im;
#pragma unroll
      for (int i = 0; i < 4; ++i) {
        re[i] = pk_bf(s[jp][8 * h + 2 * i].x, s[jp][8 * h + 2 * i + 1].x);
        im[i] = pk_bf(-s[jp][8 * h + 2 * i].y, -s[jp][8 * h + 2 * i + 1].y);
      }
      asm volatile("buffer_store_dwordx4 %0, %2, %3, 0 offen offset:%4 sc1\n\tbuffer_store_dwordx4 %1, %2, %3, %5 offen offset:%4 sc1\n\ts_nop 1"
                   ::"v"(re), "v"(im), "v"(hb[h]), "s"(rs), "n"(jp * 256 + h * 1024), "s"(8192) : "memory");
      if (T.late) sta4(ho[h] + jp * 256 + h * 1024, re), sta4(ho[h] + jp * 256 + h * 1024 + G0_STAGE, im);
      __builtin_amdgcn_sched_barrier(0);
    }
  }
  lds_signal<SW_HS>(U);
  if (T.late) {
    __builtin_amdgcn_s_setprio(0);
    wait_lgkm0();
    bar();
    bar();
    publish(ws, 4 * tm + 2, lane);
    bar();
    bar();
  } else {
    publish(ws, 4 * tm + (T.nt >> 2), lane);
    lds_signal<SW_HD>(U);
    if (wave == 0) poll_count<SW_G1>(ws, 4 * tm + 1, 16, U);
  }
}

__device__ __forceinline__ void gemm1(const Args& A, char* smem, int wave, int lane) {
  char* const ws = A.ws;
  const Tile T = tile_map();
  const int tm = T.tm, nt = T.nt;
  const uint32_t U = 4;
  const int wr = wave >> 1, wc = wave & 1, fr = lane & 15, fq = lane >> 4;
  f32x4 acc[4][4];
  if (T.late) {
    const u32x2* px = reinterpret_cast<const u32x2*>(A.xB + (tm * 128 + wr * 64 + fq * 4) * 1024 + nt * 128 + wc * 64) + fr;
    const u32x2 dw = reinterpret_cast<const u32x2*>(A.Dsk + nt * 128 + wc * 64)[fr];
    u32x2 xw[16];
#pragma unroll
    for (int i = 0; i < 16; ++i) xw[i] = px[((i >> 2) * 16 + (i & 3)) * 256];
#pragma unroll
    for (int i = 0; i < 32; ++i) {
      const uint32_t a = xw[i >> 1][i & 1], d = dw[i & 1];
      const uint32_t p = pk_bf(u2f(a << 16) * u2f(d << 16), u2f(a & 0xFFFF0000u) * u2f(d & 0xFFFF0000u));
      acc[i >> 3][(i & 1) * 2][(i >> 1) & 3] = u2f(p << 16);
      acc[i >> 3][(i & 1) * 2 + 1][(i >> 1) & 3] = u2f(p & 0xFFFF0000u);
    }
#pragma unroll
    for (int m = 0; m < 4; ++m) asm volatile("" : "+v"(acc[m][0]), "+v"(acc[m][1]), "+v"(acc[m][2]), "+v"(acc[m][3]));
    lds_wait<SW_G0E, 2>(U, 4);
#pragma nounroll
    for (int k = 0; k < 3; ++k) {
      if (k == 2) lds_wait<SW_GRP, 2>(U, 4);
      stage_c(A, wave, lane, k);
    }
    wait_vm<0>();
  } else {
#pragma unroll
    for (int i = 0; i < 16; ++i) acc[i >> 2][i & 3] = Z4;
  }
  {
    const uint64_t base = T.late ? (uint64_t)A.out : (uint64_t)(ws + WS_PS);
    __builtin_amdgcn_s_setprio(1);
    run1(acc, lds_base(smem) + G0_STAGE + wr * 8192 + ((fr >> 1) & 1) * 1024 + (8 * fq + (fr >> 2) + (fr & 2) * 2) * 16 + (fr & 1) * 8,
         lds_base(smem) + G0_STAGE + 16384 + (wc * 64 + fr) * 128 + (fq ^ (fr & 7)) * 16, T.late ? 32 - NX : NX,
         T.late ? lds_base(smem) + ((29 - NX) & 3) * G0_STAGE + wave * 8192 : 0,
         u32x4{(uint32_t)base, (uint32_t)(base >> 32) & 0xFFFFu, 16u << 20, 0x00020000u},
         T.late ? ((tm * 128 + wr * 64) * 2048 + 1024 + nt * 128 + wc * 64) * 4 : bid_x() * 32768 + wave * 8192);
  }
  if (!T.late) {
    lane = lane_id();
    lds_signal<SW_PL>(U);
    publish(ws, 64 + bid_x(), lane);
    warm_dev(smem, ws + WS_PS, bid_x() * 32768 + wave * 8192 + lane * 128);
  }
}

}

extern "C" __global__ __launch_bounds__(1024, 4) void k_all(const uint32_t* x2, const unsigned short* w_r, const unsigned short* w_i,
                                                           const unsigned short* xB, const unsigned short* Lre,
                                                           const unsigned short* Lim, const unsigned short* Bre,
                                                           const unsigned short* Bim, const unsigned short* Cre,
                                                           const unsigned short* Cim, const unsigned short* ldt,
                                                           const unsigned short* Dsk, float* out, char* ws) {
  __shared__ __attribute__((aligned(256))) char smem_[LDS_BYTES];
  char* const smem = smem_ + 256;
  const Args A{x2, w_r, w_i, xB, Lre, Lim, Bre, Bim, Cre, Cim, ldt, Dsk, out, ws};
  const int tid = tid_x(), lane = tid & 63;
  int wave = __builtin_amdgcn_readfirstlane(tid >> 6);
  const uint32_t ldsb = lds_base(smem);
  {
    const int o = lane * 128 + (lane >> 5) * ((bid_x() << 9) & 0x7000);
    uint32_t t0, t1, t2, t3;
    asm volatile(
        "s_cmp_ge_u32 %[w], 8\n\ts_cbranch_scc1 8f\n\t"
        "s_load_dwordx2 s[64:65], %[ka], 0\n\ts_load_dwordx2 s[66:67], %[ka], 24\n\ts_load_dwordx4 s[68:71], %[ka], 48\n\t"
        "s_cmp_lg_u32 %[w], 0\n\ts_cbranch_scc1 1f\n\ts_getpc_b64 s[72:73]\n\ts_add_u32 s72, s72, k_all@rel32@lo+4\n\t"
        "s_addc_u32 s73, s73, k_all@rel32@hi+12\n\ts_mov_b32 m0, %[scr]\n\ts_nop 0\n\tglobal_load_lds_dword %[o], s[72:73]\n"
        "1:\n\tv_lshrrev_b32 %0, 3, %[l]\n\tv_and_b32 %1, 7, %[l]\n\ts_lshl_b32 s72, %[w], 12\n\t"
        "s_cmp_lt_u32 %[b], 128\n\ts_cbranch_scc1 2f\n\t"
        "s_add_u32 m0, s72, %[s3]\n\ts_lshl_b32 s72, %[w], 19\n\ts_and_b32 s73, %[b], 7\n\ts_lshl_b32 s73, s73, 11\n\ts_add_u32 s72, s72, s73\n\t"
        "s_lshl_b32 s73, %[b], 4\n\ts_and_b32 s73, s73, 0x780\n\ts_add_u32 s72, s72, s73\n\t"
        "v_lshrrev_b32 %2, 1, %0\n\tv_xor_b32 %1, %1, %2\n\tv_lshlrev_b32 %1, 4, %1\n\tv_lshl_add_u32 %0, %0, 14, %1\n\t"
        "v_add_u32 %0, s72, %0\n\tv_xor_b32 %1, 64, %0\n\tv_add_u32 %1, 130048, %1\n\tv_add_u32 %2, 260096, %0\n\t"
        "v_add_u32 %3, 260096, %1\n\ts_waitcnt lgkmcnt(0)\n\t"
        "global_load_lds_dwordx4 %0, s[64:65] nt\n\tglobal_load_lds_dwordx4 %1, s[64:65] offset:1024 nt\n\t"
        "global_load_lds_dwordx4 %2, s[64:65] offset:2048 nt\n\tglobal_load_lds_dwordx4 %3, s[64:65] offset:3072 nt\n\ts_branch 9f\n"
        "2:\n\ts_add_u32 m0, s72, 256\n\ts_mul_i32 s73, %[b], 1280\n\ts_and_b32 s73, s73, 0x600\n\t"
        "s_cmp_ge_u32 %[w], 4\n\ts_cbranch_scc1 3f\n\t"
        "s_lshl_b32 s72, %[b], 17\n\ts_and_b32 s72, s72, 0x3c0000\n\ts_add_u32 s73, s73, s72\n\ts_lshl_b32 s72, %[w], 16\n\t"
        "s_add_u32 s73, s73, s72\n\tv_and_b32 %2, 3, %0\n\tv_xor_b32 %1, %1, %2\n\tv_lshlrev_b32 %1, 4, %1\n\t"
        "v_lshl_add_u32 %0, %0, 11, %1\n\tv_add_u32 %0, s73, %0\n\tv_add_u32 %1, 15360, %0\n\tv_xor_b32 %2, 64, %0\n\t"
        "v_add_u32 %3, 46080, %2\n\tv_add_u32 %2, 30720, %2\n\ts_waitcnt lgkmcnt(0)\n\t"
        "global_load_lds_dwordx4 %0, s[66:67]\n\tglobal_load_lds_dwordx4 %1, s[66:67] offset:1024\n\t"
        "global_load_lds_dwordx4 %2, s[66:67] offset:2048\n\tglobal_load_lds_dwordx4 %3, s[66:67] offset:3072\n\ts_branch 9f\n"
        "3:\n\ts_and_b32 s72, %[b], 1\n\ts_lshl_b32 s72, s72, 19\n\ts_add_u32 s73, s73, s72\n\ts_lshl_b32 s72, %[b], 12\n\t"
        "s_and_b32 s72, s72, 0x60000\n\ts_add_u32 s73, s73, s72\n\ts_and_b32 s72, %[w], 3\n\ts_lshl_b32 s72, s72, 15\n\t"
        "s_add_u32 s73, s73, s72\n\tv_xor_b32 %1, %1, %0\n\tv_lshlrev_b32 %1, 4, %1\n\tv_lshl_add_u32 %0, %0, 11, %1\n\t"
        "v_add_u32 %0, s73, %0\n\tv_add_u32 %1, 15360, %0\n\ts_waitcnt lgkmcnt(0)\n\t"
        "global_load_lds_dwordx4 %0, s[68:69]\n\tglobal_load_lds_dwordx4 %1, s[68:69] offset:1024\n\ts_add_u32 m0, m0, 2048\n\ts_nop 0\n\t"
        "global_load_lds_dwordx4 %0, s[70:71]\n\tglobal_load_lds_dwordx4 %1, s[70:71] offset:1024\n\ts_branch 9f\n"
        "8:\n\ts_cmp_lg_u32 %[w], 15\n\ts_cbranch_scc1 9f\n\ts_setprio 3\n\t"
        "s_load_dwordx4 s[64:67], %[ka], 32\n\ts_load_dwordx2 s[68:69], %[ka], 80\n\t"
        "s_and_b32 s72, %[b], 1\n\ts_lshl_b32 s72, s72, 9\n\ts_lshl_b32 s73, %[b], 2\n\ts_and_b32 s73, s73, 0x180\n\ts_add_u32 s72, s72, s73\n\t"
        "s_and_b32 s73, %[b], 128\n\ts_lshl_b32 s73, s73, 3\n\ts_add_u32 s72, s72, s73\n\tv_lshl_add_u32 %0, %[l], 2, s72\n\t"
        "s_mov_b32 m0, %[prm]\n\ts_mov_b32 exec_hi, 0\n\ts_waitcnt lgkmcnt(0)\n\tglobal_load_lds_dword %0, s[64:65]\n\t"
        "s_add_u32 m0, m0, 128\n\ts_nop 0\n\tglobal_load_lds_dword %0, s[66:67]\n\ts_add_u32 m0, m0, 128\n\ts_nop 0\n\t"
        "global_load_lds_dword %0, s[68:69]\n\ts_mov_b32 exec_hi, -1\n\ts_setprio 0\n9:"
        : "=&v"(t0), "=&v"(t1), "=&v"(t2), "=&v"(t3)
        : [l] "v"(lane), [o] "v"(o < 35096 - 16 ? o : 35096 - 16), [w] "s"(wave), [b] "s"(bid_x()),
          [ka] "s"(__builtin_amdgcn_kernarg_segment_ptr()), [scr] "n"(256 + L_SCR2), [s3] "n"(256 + 3 * G0_STAGE), [prm] "n"(256 + L_PRM + 512)
        : "s64", "s65", "s66", "s67", "s68", "s69", "s70", "s71", "s72", "s73", "scc", "memory");
  }
  if (wave < 8) {
  } else if (wave < 12) {
    const int wc = (bid_x() >> 3) & 15;
    if (bid_x() >= 128 && (wc & 7) != 3 && wave != 10)
      warm_r(A, smem, lane, wave == 11 ? (wc & 8) | 3 : wc, wave == 11 ? (wc & 7) - ((wc & 7) > 3) : wave - 8);
  } else if (wave == 13) {
    warm1(smem, Dsk, (lane & 15) * 128);
  }
  asm volatile("" : "+s"(wave));
  bool g1 = false;
  if (__builtin_expect(wave >= 12, 0)) {
    if (wave == 12) *(volatile lds_u32*)((lds_u32*)(smem + L_SYNC) + lane) = 0;
    mfma_role(A, smem, wave - 12, lane);
    g1 = bid_x() < 128;
  } else if (__builtin_expect(wave >= 8, 0)) {
    loader_role(A, smem, wave - 8, lane);
  } else {
    g1 = fft_role(A, smem, wave, lane);
  }
  if (g1) gemm1(A, smem, wave & 3, (int)__builtin_amdgcn_mbcnt_hi(~0u, __builtin_amdgcn_mbcnt_lo(~0u, 0u)));
}
#if defined(__HIP_DEVICE_COMPILE__) && __HIP_DEVICE_COMPILE__
#pragma clang attribute pop
#endif

extern "C" void kernel_launch(void* const* d_in, const int* in_sizes, int n_in, void* d_out, int out_size, void* d_ws,
                              size_t ws_size, hipStream_t stream) {
  typedef const unsigned short* P;
  k_all<<<256, 1024, 0, stream>>>((const uint32_t*)d_in[0], (P)d_in[1], (P)d_in[2], (P)d_in[3], (P)d_in[4], (P)d_in[5], (P)d_in[6],
                                  (P)d_in[7], (P)d_in[8], (P)d_in[9], (P)d_in[10], (P)d_in[11], (float*)d_out, (char*)d_ws);
}
```

```cpp
#include <hip/hip_runtime.h>
#include <stdint.h>

namespace {

#if defined(__HIP_DEVICE_COMPILE__) && __HIP_DEVICE_COMPILE__
#pragma clang attribute push(__attribute__((target("no-packed-fp32-ops"))), apply_to = function)
#endif
__device__ __forceinline__ int tid_x() { return __builtin_amdgcn_workitem_id_x(); }
__device__ __forceinline__ int bid_x() { return __builtin_amdgcn_workgroup_id_x(); }

typedef float cf __attribute__((ext_vector_type(2)));
typedef short bf16x8 __attribute__((ext_vector_type(8)));
typedef short s16x4 __attribute__((ext_vector_type(4)));
typedef _Float16 f16x8 __attribute__((ext_vector_type(8)));
typedef _Float16 f16x2 __attribute__((ext_vector_type(2)));
typedef __bf16 b16x2 __attribute__((ext_vector_type(2)));
typedef float f32x4 __attribute__((ext_vector_type(4)));
typedef uint32_t u32x4 __attribute__((ext_vector_type(4)));
typedef uint32_t u32x2 __attribute__((ext_vector_type(2)));
typedef unsigned short u16;
typedef unsigned long long u64;
typedef __attribute__((address_space(3))) uint32_t lds_u32;

__device__ __forceinline__ float u2f(uint32_t u) { return __builtin_bit_cast(float, u); }
__device__ __forceinline__ uint32_t f2u(float f) { return __builtin_bit_cast(uint32_t, f); }
__device__ __forceinline__ float bf2f(u16 b) { return u2f((uint32_t)b << 16); }
__device__ __forceinline__ float rbf(float f) { return bf2f(__builtin_bit_cast(u16, (__bf16)f)); }
__device__ __forceinline__ uint32_t pk_bf(float a, float b) {
  const b16x2 h = b16x2{(__bf16)a, (__bf16)b};
  return __builtin_bit_cast(uint32_t, h);
}
__device__ __forceinline__ uint32_t pk_h(float a, float b) {
  const f16x2 h = f16x2{(_Float16)a, (_Float16)b};
  return __builtin_bit_cast(uint32_t, h);
}
__device__ __forceinline__ cf fin(cf v) {
  float x = v.x, y = v.y;
  asm("" : "+v"(x), "+v"(y));
  return cf{x, y};
}
__device__ __forceinline__ cf cmul(cf a, cf b) {
  return cf{__builtin_fmaf(a.x, b.x, -(a.y * b.y)), __builtin_fmaf(a.x, b.y, a.y * b.x)};
}
__device__ __forceinline__ cf cmad(cf a, cf b, cf c) {
  return cf{__builtin_fmaf(a.x, b.x, __builtin_fmaf(-a.y, b.y, c.x)), __builtin_fmaf(a.x, b.y, __builtin_fmaf(a.y, b.x, c.y))};
}
__device__ __forceinline__ cf cisr(int k, float inv_n) {
  const float r = (float)k * inv_n;
  return cf{__builtin_amdgcn_cosf(r), -__builtin_amdgcn_sinf(r)};
}
template <int N> __device__ __forceinline__ cf cis(int k) { return cisr(k, 1.0f / N); }
template <int N> __device__ __forceinline__ void wait_vm() { asm volatile("s_waitcnt vmcnt(%0)" ::"n"(N) : "memory"); }
__device__ __forceinline__ void wait_lgkm0() { asm volatile("s_waitcnt lgkmcnt(0)" ::: "memory"); }
__device__ __forceinline__ void glds2_dev(const void* sb, uint32_t v0, uint32_t lds_dst) {
  uint32_t keep;
  asm volatile(
      "s_mov_b32 %0, m0\n\ts_mov_b32 m0, %3\n\ts_nop 0\n\tglobal_load_lds_dwordx4 %1, %2 sc1\n\t"
      "global_load_lds_dwordx4 %1, %2 offset:1024 sc1\n\ts_mov_b32 m0, %0"
      : "=&s"(keep)
      : "v"(v0), "s"(sb), "s"(lds_dst)
      : "memory");
}
__device__ __forceinline__ void gdev2(const void* sb, uint32_t v0, uint32_t v1, uint32_t lds_dst) {
  uint32_t keep;
  asm volatile(
      "s_mov_b32 %0, m0\n\ts_mov_b32 m0, %4\n\ts_nop 0\n\tglobal_load_lds_dwordx4 %1, %3 sc1\n\t"
      "global_load_lds_dwordx4 %2, %3 offset:1024 sc1\n\ts_mov_b32 m0, %0"
      : "=&s"(keep)
      : "v"(v0), "v"(v1), "s"(sb), "s"(lds_dst)
      : "memory");
}
__device__ __forceinline__ void glds4(const void* sb0, const void* sb1, uint32_t v0, uint32_t v1, uint32_t v2, uint32_t v3,
                                      uint32_t lds_dst) {
  uint32_t keep;
  asm volatile(
      "s_mov_b32 %0, m0\n\ts_mov_b32 m0, %7\n\ts_nop 0\n\tglobal_load_lds_dwordx4 %1, %5\n\t"
      "global_load_lds_dwordx4 %2, %5 offset:1024\n\tglobal_load_lds_dwordx4 %3, %6 offset:2048\n\t"
      "global_load_lds_dwordx4 %4, %6 offset:3072\n\ts_mov_b32 m0, %0"
      : "=&s"(keep)
      : "v"(v0), "v"(v1), "v"(v2), "v"(v3), "s"(sb0), "s"(sb1), "s"(lds_dst)
      : "memory");
}
__device__ __forceinline__ constexpr uint32_t lds_base(char*) { return 256; }
__device__ __forceinline__ void bar() {
  __builtin_amdgcn_sched_barrier(0);
  asm volatile("" ::: "memory");
  __builtin_amdgcn_s_barrier();
  asm volatile("" ::: "memory");
  __builtin_amdgcn_sched_barrier(0);
}
__device__ __forceinline__ void bar_lds() {
  asm volatile("s_waitcnt lgkmcnt(0)" ::: "memory");
  bar();
}
template <int W> __device__ __forceinline__ void lds_signal(uint32_t U) {
  asm volatile("s_mov_b64 exec, 1\n\tds_add_u32 %0, %0 offset:%1\n\ts_mov_b64 exec, -1" ::"v"(U), "n"(W * 4 - 4) : "memory");
}
template <int W> __device__ __forceinline__ uint32_t lds_peek(uint32_t U) {
  return __builtin_amdgcn_readfirstlane(*(volatile lds_u32*)(size_t)(U + (W * 4 - 4)));
}
template <int W, int SLEEP> __device__ __forceinline__ void lds_wait(uint32_t U, uint32_t n) {
  uint32_t v, c;
  if (SLEEP)
    asm volatile(
        "s_branch 2f\n1:\n\ts_sleep %5\n2:\n\tds_read_b32 %0, %2 offset:%4\n\ts_waitcnt lgkmcnt(0)\n\tv_readfirstlane_b32 %1, %0\n\t"
        "s_cmp_lt_u32 %1, %3\n\ts_cbranch_scc1 1b"
        : "=&v"(v), "=&s"(c)
        : "v"(U), "s"(n * U), "n"(W * 4 - 4), "n"(SLEEP)
        : "memory", "scc");
  else
    asm volatile(
        "1:\n\tds_read_b32 %0, %2 offset:%4\n\ts_waitcnt lgkmcnt(0)\n\tv_readfirstlane_b32 %1, %0\n\t"
        "s_cmp_lt_u32 %1, %3\n\ts_cbranch_scc1 1b"
        : "=&v"(v), "=&s"(c)
        : "v"(U), "s"(n * U), "n"(W * 4 - 4)
        : "memory", "scc");
}
__device__ __forceinline__ int lane_id() {
  int l;
  asm volatile("v_mbcnt_lo_u32_b32 %0, -1, 0\n\tv_mbcnt_hi_u32_b32 %0, -1, %0" : "=v"(l));
  return l;
}
__device__ __forceinline__ uint32_t wave_max_u32(uint32_t v) {
  asm volatile(
      "s_nop 1\n\tv_max_u32_dpp %0, %0, %0 quad_perm:[1,0,3,2] row_mask:0xf bank_mask:0xf\n\t"
      "s_nop 1\n\tv_max_u32_dpp %0, %0, %0 quad_perm:[2,3,0,1] row_mask:0xf bank_mask:0xf\n\t"
      "s_nop 1\n\tv_max_u32_dpp %0, %0, %0 row_half_mirror row_mask:0xf bank_mask:0xf\n\t"
      "s_nop 1\n\tv_max_u32_dpp %0, %0, %0 row_mirror row_mask:0xf bank_mask:0xf\n\t"
      "s_nop 1\n\tv_max_u32_dpp %0, %0, %0 row_bcast:15 row_mask:0xa bank_mask:0xf\n\t"
      "s_nop 1\n\tv_max_u32_dpp %0, %0, %0 row_bcast:31 row_mask:0xc bank_mask:0xf\n\ts_nop 1"
      : "+v"(v));
  return (uint32_t)__builtin_amdgcn_readlane((int)v, 63);
}
__device__ __forceinline__ float sel_f(uint64_t m, float a, float b) {
  float r;
  asm("v_cndmask_b32_e64 %0, %1, %2, %3" : "=v"(r) : "v"(b), "v"(a), "s"(m));
  return r;
}
__device__ __forceinline__ cf sel_c(uint64_t m, cf a, cf b) { return cf{sel_f(m, a.x, b.x), sel_f(m, a.y, b.y)}; }
__device__ __forceinline__ u64 ld_dev(const u64* p) { return __hip_atomic_load(p, __ATOMIC_RELAXED, __HIP_MEMORY_SCOPE_AGENT); }
__device__ __forceinline__ void st_dev(u64* p, u64 v) { __hip_atomic_store(p, v, __ATOMIC_RELAXED, __HIP_MEMORY_SCOPE_AGENT); }
__device__ __forceinline__ void st_dev(uint32_t* p, uint32_t v) {
  __hip_atomic_store(p, v, __ATOMIC_RELAXED, __HIP_MEMORY_SCOPE_AGENT);
}
__device__ __forceinline__ f32x4 mfma_h(u32x4 a, u32x4 b, f32x4 c) {
  return __builtin_amdgcn_mfma_f32_16x16x32_f16(__builtin_bit_cast(f16x8, a), __builtin_bit_cast(f16x8, b), c, 0, 0, 0);
}
__device__ __forceinline__ f32x4 mfma_b(u32x4 a, u32x4 b, f32x4 c) {
  return __builtin_amdgcn_mfma_f32_16x16x32_bf16(__builtin_bit_cast(bf16x8, a), __builtin_bit_cast(bf16x8, b), c, 0, 0, 0);
}
struct PQ {
  u32x4 p, q;
};
constexpr f32x4 Z4 = f32x4{0.f, 0.f, 0.f, 0.f};
constexpr unsigned SPIN_MAX = 1u << 20;

constexpr size_t WS_EPOCH = 0;
constexpr size_t WS_F1 = 4096;
constexpr size_t WS_P2 = 8192;
constexpr size_t WS_HF = 1u << 19;
constexpr size_t WS_AGG = 1u << 20;
constexpr size_t WS_ZB = 2u << 20;
constexpr size_t WS_PS = 8u << 20;
constexpr size_t WS_PG = 16u << 20;
__device__ __forceinline__ uint32_t half_of(uint32_t w, int tm, int i) { return (((w >> tm) ^ ((w >> (16 + tm)) & i)) & 1) * 8192; }
__device__ __forceinline__ char* counter(char* ws, int i) { return ws + WS_HF + i * 128; }
__device__ __forceinline__ char* ps_of(char* ws, uint32_t w, int tm, int nt, int wave) {
  const int i = nt * 4 + wave;
  return i < 30 ? ws + WS_PG + tm * (2 << 20) + (3 << 19) + i * 16384 + half_of(w, tm, wave) : ws + WS_PS + (tm * 2 + i) * 8192;
}
__device__ __forceinline__ int agg_off(int lane) { return lane * 16 + (lane >= 24 ? 128 : 0); }

constexpr int NX = 12;
constexpr int G0_STAGE = 32768;
constexpr int L_Z = 0;
constexpr int L_X = 131072;
constexpr int L_TB = L_X + 16384;
constexpr int L_WS = L_TB + 4096;
constexpr int L_PART = L_WS + 4096;
constexpr int L_GRP = L_PART + 3584;
constexpr int L_RS = L_GRP + 2048;
constexpr int L_SYNC = -256;
constexpr int L_PRM = L_RS + 1024;
constexpr int L_SCR2 = L_PRM + 1024;
constexpr int LDS_BYTES = L_SCR2 + 256 + 256;
enum { SW_GRP = 2, SW_PUB, SW_WMAX, SW_XMAX, SW_HD, SW_G1, SW_FS, SW_G0E, SW_HS, SW_LD, SW_RS, SW_F1, SW_PX, SW_FR, SW_PL, SW_P2, SW_N0, SW_N1, SW_HALF };

struct Args {
  const uint32_t* x2;
  const u16 *w_r, *w_i, *xB, *Lre, *Lim, *Bre, *Bim, *Cre, *Cim, *ldt, *Dsk;
  float* out;
  char* ws;
};

struct Tile {
  int tm, tn, nt, late;
};
__device__ __forceinline__ Tile tile_map() {
  const int bid = bid_x(), late = bid >> 7, cq = (bid & 1) * 4 + ((bid >> 5) & 3);
  return Tile{(bid >> 1) & 15, late * 8 + cq, cq, late};
}
__device__ __forceinline__ u32x4 lds4(const void* p) { return *reinterpret_cast<const u32x4*>(p); }
__device__ __forceinline__ u32x4 ldsa(uint32_t a) { return *(__attribute__((address_space(3))) u32x4*)(size_t)a; }
__device__ __forceinline__ void lds_cf2(const void* p, cf& a, cf& b) {
  const f32x4 v = *reinterpret_cast<const f32x4*>(p);
  a = cf{v[0], v[1]};
  b = cf{v[2], v[3]};
}
template <int W> __device__ __forceinline__ void group_sync(uint32_t U, uint32_t n) {
  wait_lgkm0();
  lds_signal<W>(U);
  lds_wait<W, 0>(U, n);
}
__device__ __forceinline__ int k_rot() { return (bid_x() & 6) * 2 + (bid_x() & 1) * 8; }
__device__ __forceinline__ void stage0(const Args& A, char* smem, int wave, int lane, int kt) {
  const Tile T = tile_map();
  const int prow = lane >> 3;
  const uint32_t va = (T.tm * 128 + wave * 32 + prow) * 2048 + ((lane & 7) ^ (prow & 3)) * 16;
  const uint32_t vb = (T.tn * 64 + wave * 16 + prow) * 2048 + ((lane & 7) ^ prow) * 16;
  const uint32_t dst = lds_base(smem) + wave * 4096 + (kt & 3) * G0_STAGE;
  kt = (kt + k_rot()) & 15;
  glds4(A.xB + kt * 64, A.xB + kt * 64, va, va + 15360, (va ^ 64) + 2 * 15360, (va ^ 64) + 3 * 15360, dst);
  glds4(A.Bre + kt * 64, A.Bim + kt * 64 - 1024, vb, vb + 15360, vb, vb + 15360, dst + 16384);
}
__device__ __forceinline__ int g1_kt(int late, int s) {
  const int e = late ? s - 2 : s, o = 2 * tile_map().nt, r = late ? s - 10 : s + 14 - NX;
  return (unsigned)e < 8u ? ((e + 6) & 7) + (late ? 0 : 8) : (e < 0 ? o + s : (r < o ? r : r + 2)) + 16;
}
__device__ __forceinline__ uint32_t g1_vc(int wave, int lane) {
  return (tile_map().nt * 128 + (wave >> 1) * 64 + (wave & 1) * 2 + (lane >> 3) * 4) * 2048 + ((lane & 7) ^ (lane >> 3)) * 16;
}
__device__ __forceinline__ void stage_c(const Args& A, int wave, int lane, int s) {
  const int kt = g1_kt(tile_map().late, s);
  const u16* src = ((kt & 1) ? A.Cim : A.Cre) + (kt >> 1) * 64;
  const uint32_t vc = g1_vc(wave, lane);
  glds4(src, src, vc, vc + 65536 - 1024, vc, vc + 65536 - 1024, 256 + ((s + 1) & 3) * G0_STAGE + 16384 + wave * 4096);
}
__device__ __forceinline__ void warm1(char* smem, const void* base, uint32_t off) {
  uint32_t keep;
  asm volatile("s_mov_b32 %0, m0\n\ts_mov_b32 m0, %3\n\ts_nop 0\n\tglobal_load_lds_dword %1, %2\n\ts_mov_b32 m0, %0"
               : "=&s"(keep)
               : "v"(off), "s"(base), "s"(lds_base(smem) + L_SCR2)
               : "memory");
}
__device__ __forceinline__ void warm_dev(char* smem, const void* base, uint32_t off) {
  uint32_t keep;
  asm volatile("s_mov_b32 %0, m0\n\ts_mov_b32 m0, %3\n\ts_nop 0\n\tglobal_load_lds_dword %1, %2 sc1\n\ts_mov_b32 m0, %0"
               : "=&s"(keep)
               : "v"(off), "s"(base), "s"(lds_base(smem) + L_SCR2)
               : "memory");
}
typedef short s16x2 __attribute__((ext_vector_type(2)));
__device__ __forceinline__ uint32_t pmax(uint32_t m, uint32_t u) {
  return __builtin_bit_cast(uint32_t, __builtin_elementwise_max(__builtin_bit_cast(s16x2, m), __builtin_bit_cast(s16x2, u & 0x7FFF7FFFu)));
}
template <int W> __device__ __forceinline__ void lds_max(uint32_t U, uint32_t m) {
  const uint32_t h = m << 16;
  m = h > m ? h : m;
  asm volatile(
      "s_nop 1\n\tv_max_u32_dpp %0, %0, %0 quad_perm:[1,0,3,2] row_mask:0xf bank_mask:0xf\n\t"
      "s_nop 1\n\tv_max_u32_dpp %0, %0, %0 quad_perm:[2,3,0,1] row_mask:0xf bank_mask:0xf\n\t"
      "s_nop 1\n\tv_max_u32_dpp %0, %0, %0 row_half_mirror row_mask:0xf bank_mask:0xf\n\t"
      "s_mov_b64 exec, %3\n\tds_max_u32 %1, %0 offset:%2\n\ts_mov_b64 exec, -1"
      : "+v"(m)
      : "v"(U), "n"(W * 4 - 4), "s"(0x0101010101010101ull)
      : "memory");
}
__device__ __forceinline__ void warm_r(const Args& A, char* smem, int lane, int c, int i) {
  const int bid = bid_x(), tm = ((bid >> 1) & 3) + (c & 3) * 4, nt = (bid & 1) * 4 + (c >> 2);
  uint64_t px = (uint64_t)A.xB, pbr = (uint64_t)A.Bre, pbi = (uint64_t)A.Bim;
  asm volatile("" : "+s"(px), "+s"(pbr), "+s"(pbi));
  px += (tm * 128 + (nt & 3) * 32 + (lane & 31)) * 2048;
  const uint64_t pb = ((lane & 16) ? pbi : pbr) + (nt * 64 + (c & 3) * 16 + (lane & 15)) * 2048;
  const int hi = lane >> 5;
  const bool k2 = i == 4 || i == 6;
  const int sl = i < 3 ? i + 1 : (k2 ? i - 4 : i + 1) + hi;
  const uint64_t pfp = ((k2 || (i < 3 && hi)) ? pb + (k2 ? 1 << 20 : 0) : px) + ((sl + k_rot()) & 15) * 128;
  uint32_t keep;
  asm volatile("s_mov_b32 %0, m0\n\ts_mov_b32 m0, %2\n\ts_nop 0\n\tglobal_load_lds_dword %1, off\n\ts_mov_b32 m0, %0"
               : "=&s"(keep)
               : "v"(pfp), "s"(lds_base(smem) + L_SCR2)
               : "memory");
}
__device__ __forceinline__ uint32_t pk_mul_h(uint32_t a, uint32_t b) {
  return __builtin_bit_cast(uint32_t, __builtin_bit_cast(f16x2, a) * __builtin_bit_cast(f16x2, b));
}
__device__ __forceinline__ u32x2 lds_tr(uint32_t a) {
  return __builtin_bit_cast(u32x2, __builtin_amdgcn_ds_read_tr16_b64_v4i16((__attribute__((address_space(3))) s16x4*)(size_t)a));
}
__device__ __forceinline__ void sta4(uint32_t a, u32x4 v) { *(__attribute__((address_space(3))) u32x4*)(size_t)a = v; }
template <int P> __device__ __forceinline__ void prio(bool on) {
  __builtin_amdgcn_sched_barrier(0);
  if (on) __builtin_amdgcn_s_setprio(P);
  __builtin_amdgcn_sched_barrier(0);
}
__device__ __forceinline__ void row_head(u32x4 (&x)[2][2], const PQ& m16, const PQ& m1, const cf (&tw)[2][4], bool on) {
  f32x4 dr[2][2], di[2][2];
#pragma unroll
  for (int k = 0; k < 2; ++k) {
    const f32x4 ur = mfma_h(x[k][0], m16.p, Z4), ui = mfma_h(x[k][0], m16.q, Z4);
    const u32x4 xn = x[k][1] ^ 0x80008000u;
    dr[k][0] = mfma_h(x[k][1], m1.p, ur), di[k][0] = mfma_h(x[k][1], m1.q, ui);
    dr[k][1] = mfma_h(xn, m1.p, ur), di[k][1] = mfma_h(xn, m1.q, ui);
  }
  prio<0>(on);
#pragma unroll
  for (int i = 0; i < 16; ++i) {
    const int k = i >> 3, t = (i >> 2) & 1, r = i & 3;
    const cf v = fin(cmul(cf{dr[k][t][r], di[k][t][r]}, tw[t][r]));
    dr[k][t][r] = v.x, di[k][t][r] = v.y;
  }
  prio<1>(on);
#pragma unroll
  for (int i = 0; i < 16; ++i) x[i >> 3][(i >> 2) & 1][i & 3] = pk_h(dr[i >> 3][(i >> 2) & 1][i & 3], di[i >> 3][(i >> 2) & 1][i & 3]);
}
template <int W, bool B = false> __device__ __forceinline__ void poll_count(char* p, uint32_t n, uint32_t U) {
  uint32_t c0, c1, c2, t, lim = SPIN_MAX;
#define ASK(c) "global_atomic_add " c ", %5, %5, %6 sc0\n\t"
#define CHK(c) "s_waitcnt vmcnt(%10)\n\tv_readfirstlane_b32 %3, " c "\n\ts_cmp_ge_u32 %3, %7\n\ts_cbranch_scc1 2f\n\t" ASK(c)
  asm volatile(
      "s_mov_b64 exec, 1\n\t" ASK("%0") "s_sleep 4\n\t" ASK("%1") "s_sleep 4\n\t" ASK("%2") ".if %11\n\ts_barrier\n.endif\n1:\n\t" CHK("%0") CHK("%1") CHK("%2")
      "s_sub_u32 %4, %4, 1\n\ts_cbranch_scc0 1b\n2:\n\tds_add_u32 %8, %8 offset:%9\n\ts_mov_b64 exec, -1\n\ts_waitcnt vmcnt(0)"
      : "=&v"(c0), "=&v"(c1), "=&v"(c2), "=&s"(t), "+s"(lim)
      : "v"(0), "s"(p), "s"(n), "v"(U), "n"(W * 4 - 4), "n"(2), "n"(B)
      : "memory", "scc");
#undef ASK
#undef CHK
}
__device__ __forceinline__ bool fft_role(const Args& A, char* smem, int f, int lane) {
  char* const ws = A.ws;
  const Tile T = tile_map();
  const int bid = bid_x(), li = bid & 127;
  int l15 = lane & 15, q = lane >> 4;
  lds_u32* const sw = (lds_u32*)(smem + L_SYNC);
  const uint32_t U = (uint32_t)(size_t)(sw + 1), ldsb = lds_base(smem);
  const __amdgpu_buffer_rsrc_t rzb = __builtin_amdgcn_make_buffer_rsrc((void*)(ws + WS_ZB), 0, 48 << 20, 0x00020000);
  auto fsync = [&](int n) { group_sync<SW_FS>(U, 8 * n); };
  auto bars = [&](int n) {
#pragma nounroll
    for (int i = 0; i < n; ++i) bar();
  };
  u64 epoch = 0;
  if (f == 0) epoch = ld_dev((const u64*)(ws + WS_EPOCH));
  if (f == 7) {
    const __amdgpu_buffer_rsrc_t ra = __builtin_amdgcn_make_buffer_rsrc((void*)(ws + WS_AGG), 0, 1 << 20, 0x00020000);
    __builtin_amdgcn_raw_buffer_store_b128(u32x4{0u, 0u, 0u, 0u}, ra, (T.tm * 32 + T.tn) * 2048 + agg_off(lane), 0, 16);
    __builtin_amdgcn_raw_buffer_store_b128(u32x4{0u, 0u, 0u, 0u}, ra, (T.tm * 32 + 16 + T.tn) * 2048 + agg_off(lane), 0, 16);
    if (!T.late && lane < (T.tn ? 1 : 4)) st_dev((uint32_t*)counter(ws, lane ? 4 * T.tm + lane - 1 : 64 + li), 0u);
    if (lane == 0) st_dev((uint32_t*)(ws + WS_P2) + bid, 0u);
  }
  __builtin_amdgcn_sched_barrier(0);
  auto ks_of = [&](int slot) {
    const int w = slot & 7, k = bid ? bid + 256 * w : (w < 4 ? 256 * w : 256 * w - 896);
    return slot < 8 ? k : (k ? 2048 - k : 1024);
  };
  {
    const int e = f & 3, n = 4 * q + e;
    uint32_t* tb = reinterpret_cast<uint32_t*>(smem + L_TB) + lane * 4;
    uint32_t* tx = reinterpret_cast<uint32_t*>(smem + L_X) + lane * 4;
    cf c = cis<256>(n * l15 * (f < 4 ? 16 : 1));
    if (f < 4) {
      c = c * 0.25f;
      tb[e] = pk_h(c.x, -c.y);
      tb[256 + e] = pk_h(c.y, c.x);
      if (T.late) {
        const uint32_t ph = pk_bf(c.x, -c.y), qh = pk_bf(c.y, c.x);
        const float lr = c.x - u2f(ph << 16), lo = c.y - u2f(qh << 16);
        tx[e] = ph;
        tx[256 + e] = qh;
        tx[512 + e] = pk_bf(lr, -lo);
        tx[768 + e] = pk_bf(lo, lr);
      } else {
        const cf d = cis<32>((2 * n + 1) * l15) * 0.25f;
        tx[e] = pk_h(d.x, -d.y);
        tx[256 + e] = pk_h(d.y, d.x);
      }
    } else {
      *reinterpret_cast<cf*>(tb + 512 + (e >> 1) * 256 + (e & 1) * 2) = c;
    }
    if (!T.late) {
      *reinterpret_cast<cf*>(tx + 512 + (f >> 1) * 256 + (f & 1) * 2) = cis<512>(n * (16 * (f >> 2) + l15)) * 0.5f;
      const cf w = cis<1024>(l15 + 16 * (f >> 2) + 32 * n);
      *reinterpret_cast<cf*>(tx + 1536 + (f >> 1) * 256 + (f & 1) * 2) = cf{w.y, -w.x};
      const int slot = f < 4 ? l15 : n, u = f < 4 ? q >> 1 : l15 & 1, j = f < 4 ? n & 7 : l15 >> 1;
      const cf g = cis<2048>(j * ks_of(slot)) * (((bid ? slot >> 3 : (slot >> 2) & 1) == u) ? 0.5f : 0.f);
      const uint32_t cj = slot < 8 ? 0u : 0x80000000u;
      tx[2560 + (f >> 2) * 512 + e] = pk_h(g.x, -g.y) ^ (f < 4 ? 0u : cj);
      tx[2816 + (f >> 2) * 512 + e] = pk_h(g.y, g.x) ^ (f < 4 ? cj | cj >> 16 : cj);
    }
  }
  if (!T.late) {
    if (f == 0) wait_vm<1>(); else if (f == 7) wait_vm<4>(); else wait_vm<0>();
  }
  bar_lds();
  PQ m16, m1, m8;
  cf tw[2][4];
  u32x4 wv[4];
  uint32_t pay = 0;
  m16.p = lds4(smem + L_TB + lane * 16), m16.q = lds4(smem + L_TB + 1024 + lane * 16);
  if (T.late) {
    const char* tab = smem + L_X + lane * 16;
    const uint32_t xl = ldsb + 3 * G0_STAGE + f * 4096 + lane * 16;
    const uint32_t xc = ldsb + 3 * G0_STAGE + (l15 + 64 * q) * 128 + (f ^ (l15 >> 1)) * 16;
    PQ mh, ml;
    mh.p = lds4(tab), mh.q = lds4(tab + 1024), ml.p = lds4(tab + 2048), ml.q = lds4(tab + 3072);
    lds_cf2(smem + L_TB + 2048 + lane * 16, tw[0][0], tw[0][1]), lds_cf2(smem + L_TB + 3072 + lane * 16, tw[0][2], tw[0][3]);
    if (f == 0) wait_vm<1>(); else if (f == 7) wait_vm<3>(); else wait_vm<0>();
    {
      uint32_t mx[2] = {0, 0};
#pragma unroll
      for (int i = 0; i < 4; ++i) wv[i] = ldsa(xl + i * 1024);
#pragma unroll
      for (int i = 0; i < 16; ++i) mx[i & 1] = pmax(mx[i & 1], wv[i >> 2][i & 3]);
      lds_max<SW_XMAX>(U, pmax(mx[0], mx[1]));
    }
    fsync(1);
#pragma unroll
    for (int e = 0; e < 4; ++e) wv[e] = ldsa(xc + e * 2048);
    int ecu = (int)(lds_peek<SW_XMAX>(U) >> 23);
    ecu = ecu < 26 ? 26 : ecu;
    pay = (uint32_t)ecu;
    float sc = u2f((uint32_t)(257 - ecu) << 23);
    asm volatile("" : "+v"(sc));
#pragma unroll
    for (int r = 0; r < 4; ++r) tw[0][r] = tw[0][r] * sc;
    f32x4 dr[4], di[4];
#pragma unroll
    for (int c = 0; c < 4; ++c) {
      const u32x4 x = u32x4{wv[0][c], wv[1][c], wv[2][c], wv[3][c]};
      dr[c] = mfma_b(x, mh.p, mfma_b(x, ml.p, Z4));
      di[c] = mfma_b(x, mh.q, mfma_b(x, ml.q, Z4));
    }
#pragma unroll
    for (int i = 0; i < 16; ++i) {
      const cf v = fin(cmul(cf{dr[i >> 2][i & 3], di[i >> 2][i & 3]}, tw[0][i & 3]));
      dr[i >> 2][i & 3] = v.x, di[i >> 2][i & 3] = v.y;
    }
#pragma unroll
    for (int c = 0; c < 4; ++c) {
      const u32x4 a = u32x4{pk_h(dr[c][0], di[c][0]), pk_h(dr[c][1], di[c][1]), pk_h(dr[c][2], di[c][2]), pk_h(dr[c][3], di[c][3])};
      dr[c] = mfma_h(m16.p, a, Z4);
      di[c] = mfma_h(m16.q, a, Z4);
    }
#pragma unroll
    for (int r = 0; r < 4; ++r)
      sta4(xc + r * 2048, u32x4{pk_h(dr[0][r], di[0][r]), pk_h(dr[1][r], di[1][r]), pk_h(dr[2][r], di[2][r]), pk_h(dr[3][r], di[3][r])});
    fsync(2);
#pragma unroll
    for (int i = 0; i < 4; ++i) wv[i] = ldsa(xl + i * 1024);
    const int zo = (32 * f + (lane >> 3)) * 16384 + (li & 7) * 2048 + (li >> 3) * 128 + ((lane & 7) ^ q) * 16;
    asm volatile("" : "+v"(wv[0]), "+v"(wv[1]), "+v"(wv[2]), "+v"(wv[3]));
#pragma unroll
    for (int i = 0; i < 4; ++i) __builtin_amdgcn_raw_buffer_store_b128(wv[i], rzb, (zo ^ ((i & 1) * 64)) + i * 131072, 0, 16);
    bar();
  } else {
    const char* tab = smem + L_X + lane * 16;
    m1.p = lds4(tab), m1.q = lds4(tab + 1024), m8.p = lds4(tab + 10240), m8.q = lds4(tab + 11264);
#pragma unroll
    for (int i = 0; i < 4; ++i) lds_cf2(tab + 2048 + i * 1024, tw[i >> 1][(i & 1) * 2], tw[i >> 1][(i & 1) * 2 + 1]);
  }
  wait_vm<0>();
  asm volatile("" : "+v"(epoch));
  const u64 tag = ((epoch + 1) & 0x7FFFFFFFFFFFull) | 0x800000000000ull;
  if (T.late && f == 0) pay |= lds_peek<SW_N1>(U) > lds_peek<SW_N0>(U) ? 256u : 0u;
  u64 f1 = (tag << 16) | (u64)pay;
  asm volatile("" : "+v"(f1));
  bar();
  if (f == 0 && lane == 0) st_dev((u64*)(ws + WS_F1) + bid, f1);
  int nb = f == 0 && !T.late ? 10 : 11;
  if (T.late && f >= 6) {
    uint64_t pbr = (uint64_t)A.Bre, pbi = (uint64_t)A.Bim;
    asm volatile("" : "+s"(pbr), "+s"(pbi));
    uint64_t pf = ((lane & 16) ? pbi : pbr) + (T.tn * 64 + (T.tm >> 2) * 16 + l15) * 2048 + ((2 * f + (lane >> 5) + k_rot()) & 15) * 128;
    asm volatile("" : "+v"(pf));
    bars(f - 3);
    nb -= f - 3;
    __builtin_amdgcn_s_sleep(5);
    uint32_t keep;
    asm volatile("s_mov_b32 %0, m0\n\ts_mov_b32 m0, %2\n\ts_nop 0\n\tglobal_load_lds_dword %1, off\n\ts_mov_b32 m0, %0"
                 : "=&s"(keep)
                 : "v"(pf), "s"(ldsb + L_SCR2)
                 : "memory");
  }
  bars(nb);
  int zo = (q < 2 ? bid : (bid ? 256 - bid : 128)) * 16384 + (q & 1) * 8192 + f * 256 + l15 * 16;
  asm volatile("" : "+v"(zo));
  uint32_t ex = 0, e2 = 0, e3 = 0;
  if (f == 0) {
    u64 v[4];
    auto poll1 = [&]() {
#pragma unroll
      for (int i = 0; i < 4; ++i) v[i] = ld_dev((const u64*)(ws + WS_F1) + lane + 64 * i);
    };
    __builtin_amdgcn_s_sleep(5);
    poll1();
    if (!T.late) bar();
    bar();
    __builtin_amdgcn_s_setprio(2);
#pragma nounroll
    for (unsigned spins = 0; spins < SPIN_MAX; ++spins) {
      bool ok = true;
#pragma unroll
      for (int i = 0; i < 4; ++i) ok &= (v[i] >> 16) == tag;
      if (__builtin_amdgcn_ballot_w64(!ok) == 0) break;
      __builtin_amdgcn_s_sleep(16);
      poll1();
    }
    lds_signal<SW_F1>(U);
    e2 = (uint32_t)v[2] & 511u, e3 = (uint32_t)v[3] & 511u;
    asm volatile("" : "+v"(e2), "+v"(e3));
  } else {
    if (T.late && f >= 4) {
      __builtin_amdgcn_s_sleep(6);
      warm1(smem, (f & 1) ? A.Cre : A.Cim, (T.nt * 32 + T.tm * 2 + ((f >> 1) & 1)) * 8192 + lane * 128);
    }
    bar();
  }
  u32x4 zi[4];
  if (!T.late) {
    if (f) lds_wait<SW_F1, 1>(U, 1);
#pragma unroll
    for (int e = 0; e < 4; ++e) zi[e] = __builtin_amdgcn_raw_buffer_load_b128(rzb, zo + e * 2048, 0, 16);
  }
  if (f == 0) {
    const uint64_t b2 = __builtin_amdgcn_ballot_w64(e2 >= 256u), b3 = __builtin_amdgcn_ballot_w64(e3 >= 256u);
    auto two_of = [&](uint32_t w) { return __builtin_popcount((w >> 2 * l15) & 3u); };
    const int n = two_of(b2) + two_of(b2 >> 32) + two_of(b3) + two_of(b3 >> 32);
    const uint32_t hw = ((uint32_t)__builtin_amdgcn_ballot_w64(n >= 6) & 0xFFFFu) | (uint32_t)__builtin_amdgcn_ballot_w64(n > 2 && n < 6) << 16;
    *(volatile lds_u32*)(sw + SW_HALF) = hw;
    e2 &= 255u, e3 &= 255u;
    ex = wave_max_u32(e2 > e3 ? e2 : e3);
    const float s2 = __builtin_amdgcn_ldexpf(1.0f, (int)e2 - (int)ex), s3 = __builtin_amdgcn_ldexpf(1.0f, (int)e3 - (int)ex);
    uint32_t* rs = reinterpret_cast<uint32_t*>(smem + L_RS) + lane;
    rs[0] = pk_h(s2, s2);
    rs[64] = pk_h(s3, s3);
    wait_lgkm0();
    lds_signal<SW_RS>(U);
    __builtin_amdgcn_s_setprio(0);
  }
  if (f == 0 && lane == 0) {
    if (T.late) st_dev((uint32_t*)(ws + WS_P2) + bid, 256u);
    if (bid == 0) st_dev((u64*)(ws + WS_EPOCH), epoch + 1);
  }
  if ((bid | f) == 0) __builtin_amdgcn_s_setprio(3);
  if (!T.late) {
    const __amdgpu_buffer_rsrc_t rr = __builtin_amdgcn_make_buffer_rsrc((void*)A.w_r, 0, 2048 * 1026, 0x00020000);
    const __amdgpu_buffer_rsrc_t ri = __builtin_amdgcn_make_buffer_rsrc((void*)A.w_i, 0, 2048 * 1026, 0x00020000);
    uint32_t sr, si;
    auto wload = [&](int ka) {
      const int oa = ka * 1026, ob = ((2048 - ka) & 2047) * 1026, o1 = (ka & 1) ? oa - 2 : ob, o0 = (ka & 1) ? ob + 2 : oa;
      wv[0] = __builtin_amdgcn_raw_buffer_load_b128(rr, lane * 16, o1, 2);
      wv[1] = __builtin_amdgcn_raw_buffer_load_b128(ri, lane * 16, o1, 2);
      wv[2] = __builtin_amdgcn_raw_buffer_load_b128(rr, lane * 16, o0, 2);
      wv[3] = __builtin_amdgcn_raw_buffer_load_b128(ri, lane * 16, o0, 2);
      const int so = (int)f2u(sel_f(1, u2f(ob), sel_f(2, u2f(oa + 1024), u2f(o1 + 1024))));
      sr = __builtin_amdgcn_raw_buffer_load_b16(rr, so, 0, 2);
      si = __builtin_amdgcn_raw_buffer_load_b16(ri, so, 0, 2);
    };
    const bool two = (bid | f) == 0;
    u32x4 w2r, w2i;
    uint32_t s2r, s2i;
    if (two) {
      wload(1024);
      w2r = wv[2], w2i = wv[3], s2r = sr, s2i = si;
    }
    wload(ks_of(f));
    const uint32_t row = ldsb + L_Z + f * 2048;
    uint32_t wa = row, wb = two ? ldsb + L_X + 14336 : row + 16384;
    uint32_t ra = row + q * 512 + ((l15 >> 2) ^ f ^ ((q & 1) << 2)) * 16 + (l15 & 3) * 4;
    uint32_t w1 = ((bid & 1) ? wa : wb) + lane * 16, w0 = ((bid & 1) ? wb : wa) + lane * 16;
    uint32_t za = ldsb + L_Z + l15 * 2048 + f * 256 + ((4 * q) ^ (l15 & 7) ^ ((f & 2) << 1)) * 16;
    asm volatile("" : "+v"(ra), "+v"(w1), "+v"(w0), "+v"(za));
    __builtin_amdgcn_sched_barrier(0);
    lds_wait<SW_RS, 1>(U, 1);
    {
      const u32x4 rs = lds4(smem + L_RS + f * 64 + (l15 >> 3) * 32 + (q & 1) * 16);
      u32x4 o[4];
      asm volatile("" : "+v"(zi[0]), "+v"(zi[1]), "+v"(zi[2]), "+v"(zi[3]));
#pragma unroll
      for (int c = 0; c < 4; ++c) {
        u32x4 a;
#pragma unroll
        for (int e = 0; e < 4; ++e) a[e] = pk_mul_h(zi[e][c], rs[e]);
        const f32x4 re = mfma_h(a, m8.p, Z4), im = mfma_h(a, m8.q, Z4);
#pragma unroll
        for (int r = 0; r < 4; ++r) o[r][c] = pk_h(re[r], im[r]);
      }
#pragma unroll
      for (int r = 0; r < 4; ++r) sta4(za ^ (r * 16), o[r]);
    }
    __builtin_amdgcn_sched_barrier(0);
    uint32_t wsp;
    {
      auto shift = [](u32x4& w, uint32_t last) {
        const uint32_t nx = (uint32_t)__builtin_amdgcn_update_dpp((int)last, (int)(uint32_t)w[0], 0x130, 0xF, 0xF, false);
        w = u32x4{__builtin_amdgcn_alignbit(w[1], w[0], 16), __builtin_amdgcn_alignbit(w[2], w[1], 16),
                  __builtin_amdgcn_alignbit(w[3], w[2], 16), __builtin_amdgcn_alignbit(nx, w[3], 16)};
      };
      uint32_t mx[4] = {0, 0, 0, 0};
#pragma unroll
      for (int i = 0; i < 8; ++i) mx[i & 1] = pmax(mx[i & 1], wv[2 + (i >> 2)][i & 3]);
      shift(wv[0], sr), shift(wv[1], si);
      wsp = sr | si << 16;
      if (two) {
        sta4(ldsb + L_WS + lane * 16, w2r), sta4(ldsb + L_WS + 1024 + lane * 16, w2i);
#pragma unroll
        for (int i = 0; i < 4; ++i) mx[0] = pmax(mx[0], w2r[i]), mx[1] = pmax(mx[1], w2i[i]);
        shift(w2r, s2r), shift(w2i, s2i);
        sta4(ldsb + L_WS + 2048 + lane * 16, w2r), sta4(ldsb + L_WS + 3072 + lane * 16, w2i);
        wsp = (uint32_t)__builtin_amdgcn_update_dpp((int)wsp, (int)(s2r | s2i << 16), 0x114, 0xF, 0x2, false);
      }
#pragma unroll
      for (int i = 0; i < 8; ++i) mx[2 + (i & 1)] = pmax(mx[2 + (i & 1)], wv[i >> 2][i & 3]);
      lds_max<SW_WMAX>(U, pmax(pmax(pmax(mx[0], wsp), mx[1]), pmax(mx[2], mx[3])));
    }
    fsync(1);
    prio<1>(!two);
    u32x4 x[2][2];
    auto rows = [&]() {
#pragma unroll
      for (int i = 0; i < 16; ++i) x[i >> 3][(i >> 2) & 1][i & 3] = *(lds_u32*)(size_t)((ra ^ ((i & 4) * 16)) + (i & 3) * 128 + (i >> 3) * 16384);
    };
    rows();
    pay = lds_peek<SW_WMAX>(U) >> 23;
    pay = pay < 26u ? 26u : pay;
    float fs = __builtin_amdgcn_ldexpf(0.25f, 125 - (int)pay);
    if (two) x[1][0] = x[0][0] ^ 0x80000000u, x[1][1] = x[0][1] ^ 0x80000000u;
    asm volatile("" : "+v"(x[0][0]), "+v"(x[0][1]), "+v"(x[1][0]), "+v"(x[1][1]), "+v"(fs)::"memory");
    sta4(w1, wv[0]);
    sta4(w1 + 1024, wv[1]);
    sta4(w0, wv[2]);
    sta4(w0 + 1024, wv[3]);
#pragma nounroll
    for (int jb = 0;; jb = 4) {
      u32x2 fw[4][2];
#pragma unroll
      for (int h = 0; h < 2; ++h) {
        fw[0][h] = lds_tr(wa + q * 256 + l15 * 8 + h * 128);
        fw[1][h] = lds_tr(wa + q * 256 + l15 * 8 + h * 128 + 1024);
        fw[2][h] = lds_tr(wb + (3 - q) * 256 + l15 * 8 + h * 128);
        fw[3][h] = lds_tr(wb + (3 - q) * 256 + l15 * 8 + h * 128 + 1024);
      }
      row_head(x, m16, m1, tw, !two);
#pragma unroll
      for (int i = 0; i < 8; ++i)
        fw[2 + (i >> 2)][(i >> 1) & 1][i & 1] =
            (uint32_t)__builtin_amdgcn_update_dpp(0, (int)(uint32_t)fw[2 + (i >> 2)][(i >> 1) & 1][i & 1], 0x140, 0xF, 0xF, true);
      const uint32_t s0 = __builtin_amdgcn_readlane(wsp, jb), s1 = __builtin_amdgcn_readlane(wsp, jb + 1);
#pragma unroll
      for (int t = 0; t < 2; ++t) {
        cf u[4];
        lds_cf2(smem + L_X + 6144 + t * 2048 + lane * 16, u[0], u[1]), lds_cf2(smem + L_X + 7168 + t * 2048 + lane * 16, u[2], u[3]);
        f32x4 ar = mfma_h(m16.p, x[0][t], Z4), ai = mfma_h(m16.q, x[0][t], Z4), br = mfma_h(m16.p, x[1][t], Z4), bi = mfma_h(m16.q, x[1][t], Z4);
        auto half = [](uint32_t w, int hi) { return u2f(hi ? w & 0xFFFF0000u : w << 16); };
        cf lo[4];
#pragma unroll
        for (int r = 0; r < 4; ++r) {
          const int rr = t ? 3 - r : r;
          lo[r] = fin(cf{half(fw[2 * t][rr >> 1][rr & 1], 0), half(fw[2 * t + 1][rr >> 1][rr & 1], 0)});
        }
        prio<0>(!two);
#pragma unroll
        for (int r = 0; r < 4; ++r) {
          const int rr = t ? r : 3 - r;
          const cf hi = cf{half(fw[2 - 2 * t][rr >> 1][rr & 1], 1), half(fw[3 - 2 * t][rr >> 1][rr & 1], 1)};
          cf fa = t ? hi : lo[r], fb = t ? lo[r] : hi;
          fb.y = -fb.y;
          if (t == 0 && r == 0 && lane == 0) {
            fa = (fa + cf{half(s0, 0), -half(s0, 1)}) * 0.5f;
            fb = (fb + cf{half(s1, 0), half(s1, 1)}) * 0.5f;
          }
          const cf tb = cf{br[r] * fs, bi[r] * fs};
          const cf E = cf{__builtin_fmaf(ar[r], fs, tb.x), __builtin_fmaf(ai[r], fs, tb.y)};
          const cf D = cf{__builtin_fmaf(ar[r], fs, -tb.x), __builtin_fmaf(ai[r], fs, -tb.y)};
          const cf X1 = cmad(u[r], D, E), X2 = cf{__builtin_fmaf(2.f, E.x, -X1.x), __builtin_fmaf(2.f, E.y, -X1.y)};
          const cf Yb = cmul(X2, fb), S = cmad(X1, fa, Yb);
          const cf Df = cf{__builtin_fmaf(-2.f, Yb.x, S.x), __builtin_fmaf(-2.f, Yb.y, S.y)};
          const cf O1 = fin(cmad(cf{u[r].x, -u[r].y}, Df, S));
          const cf O2 = fin(cf{__builtin_fmaf(2.f, S.x, -O1.x), __builtin_fmaf(-2.f, S.y, O1.y)});
          ar[r] = O1.x, ai[r] = O1.y, br[r] = O2.x, bi[r] = O2.y;
        }
        prio<1>(!two);
#pragma unroll
        for (int r = 0; r < 4; ++r) x[0][t][r] = pk_h(ar[r], -ai[r]), x[1][t][r] = pk_h(br[r], bi[r]);
      }
      if (!two) break;
      if (jb) {
        x[0][0] = ldsa(row + lane * 16), x[0][1] = ldsa(row + 1024 + lane * 16);
        break;
      }
      sta4(row + lane * 16, x[0][0]), sta4(row + 1024 + lane * 16, x[0][1]);
      rows();
      x[0][0] = x[1][0] ^ 0x80000000u, x[0][1] = x[1][1] ^ 0x80000000u;
      wa = ldsb + L_WS, wb = ldsb + L_WS + 2048;
    }
    row_head(x, m16, m1, tw, !two);
    const uint32_t oa = row + l15 * 128 + (q ^ (l15 & 7)) * 16;
#pragma unroll
    for (int i = 0; i < 4; ++i) {
      const f32x4 re = mfma_h(x[i >> 1][i & 1], m16.p, Z4), im = mfma_h(x[i >> 1][i & 1], m16.q, Z4);
      sta4((oa ^ ((i & 1) * 64)) + (i >> 1) * 16384, u32x4{pk_h(re[0], im[0]), pk_h(re[1], im[1]), pk_h(re[2], im[2]), pk_h(re[3], im[3])});
    }
    m8.p = lds4(smem + L_X + 12288 + lane * 16), m8.q = lds4(smem + L_X + 13312 + lane * 16);
    fsync(2);
    u32x4 yi[4];
#pragma unroll
    for (int e = 0; e < 4; ++e) yi[e] = lds4(smem + L_Z + (4 * q + e) * 2048 + f * 256 + (l15 ^ ((2 * f + (l15 >> 3)) & 7)) * 16);
    asm volatile("" : "+v"(yi[0]), "+v"(yi[1]), "+v"(yi[2]), "+v"(yi[3]));
    u32x4 o[4];
#pragma unroll
    for (int c = 0; c < 4; ++c) {
      const u32x4 y = u32x4{yi[0][c], yi[1][c], yi[2][c], yi[3][c]};
      const f32x4 re = mfma_h(m8.p, y, Z4), im = mfma_h(m8.q, y, Z4);
#pragma unroll
      for (int r = 0; r < 4; ++r) o[r][c] = pk_h(re[r], im[r]);
    }
    const int ct = (8 * f + 4 * (l15 >> 3) + q) & 15, k1 = bid ? 256 - bid : 128;
    const int zc = (int)(WS_PG - WS_ZB) + ct * (2 << 20) + (1 << 20) + (f >> 1) * 65536 + (l15 & 7) * 16;
    const uint32_t hw = lds_peek<SW_HALF>(U);
    int z0 = zc + half_of(hw, ct, bid >> 6), z1 = zc + half_of(hw, ct, k1 >> 6);
    asm volatile("" : "+v"(o[0]), "+v"(o[1]), "+v"(o[2]), "+v"(o[3]), "+v"(z0), "+v"(z1));
    lds_wait<SW_HD, 1>(U, 4);
#pragma unroll
    for (int i = 0; i < 4; ++i) {
      const int k = (i & 1) ? k1 : bid;
      __builtin_amdgcn_raw_buffer_store_b128(o[i], rzb, ((i & 1) ? z1 : z0) + (i >> 1) * 262144 + (k >> 6) * 16384 + (k & 63) * 128, 0, 16);
    }
    __builtin_amdgcn_s_setprio(0);
  } else {
    if (f >= 4) return true;
    if (f == 1) {
      lds_wait<SW_PUB, 4>(U, 4);
      lds_wait<SW_F1, 4>(U, 1);
      __builtin_amdgcn_s_sleep(10);
      poll_count<SW_G1>(counter(ws, 4 * T.tm), 16, U);
    }
  }
  const int N = T.late ? 32 - NX : NX;
  if (T.late && f != 1) return false;
  bars(2);
  wait_vm<0>();
  __builtin_amdgcn_s_waitcnt(0x0F70);
  bar();
  lane = lane_id();
  uint32_t vo = T.tm * (2 << 20) + (1 << 20) + T.nt * 65536 + (f >> 1) * 16384 + half_of(lds_peek<SW_HALF>(U), T.tm, f >> 1) + (f & 1) * 4096 +
                (lane >> 3) * 128 + ((lane & 7) ^ (lane >> 4)) * 16;
  uint32_t vp = vo ^ 64;
  asm volatile("" : "+v"(vo), "+v"(vp));
  u32x4 pw;
  auto scales = [&]() {
    uint32_t mw = pw[0] > pw[1] ? pw[0] : pw[1], m2 = pw[2] > pw[3] ? pw[2] : pw[3];
    mw = wave_max_u32(mw > m2 ? mw : m2);
#pragma unroll
    for (int i = 0; i < 4; ++i) {
      const float sc = __builtin_amdgcn_ldexpf(1.0f, (int)(uint32_t)pw[i] - (int)mw);
      pw[i] = pk_h(sc, sc);
    }
    if (lane == 32) pw[0] = ex + mw;
    if (lane < 33) sta4(ldsb + L_RS + lane * 16, pw);
    wait_lgkm0();
    lds_signal<SW_RS>(U);
  };
  if (f == 1) {
    bars(T.late ? 3 : 1);
    {
      uint32_t c;
      asm volatile("s_mov_b64 exec, 1\n\tglobal_atomic_add %0, %1, %1, %2 sc0\n\ts_mov_b64 exec, -1\n\ts_barrier\n\ts_waitcnt vmcnt(0)"
                   : "=&v"(c)
                   : "v"(0), "s"(counter(ws, 4 * T.tm + 2))
                   : "memory");
      if (__builtin_amdgcn_readfirstlane(c) < 32u) poll_count<SW_FR>(counter(ws, 4 * T.tm + 2), 32, U);
    }
    if (T.late) {
      bars(N - 10);
      poll_count<SW_PX, true>(counter(ws, 64 + li), 4, U);
      return false;
    }
    bars(N - 7);
  } else if (f == 0) {
    if (lane == 0) st_dev((uint32_t*)(ws + WS_P2) + bid, 256u | pay);
    bars(N - 7);
    const __amdgpu_buffer_rsrc_t rp = __builtin_amdgcn_make_buffer_rsrc((void*)(ws + WS_P2), 0, 1024, 0x00020000);
    pw = __builtin_amdgcn_raw_buffer_load_b128(rp, lane * 16, 0, 16);
    bars(2);
#pragma nounroll
    for (unsigned spins = 0; spins < SPIN_MAX; ++spins) {
      if (__builtin_amdgcn_ballot_w64((pw[0] & pw[1] & pw[2] & pw[3] & 256u) == 0) == 0) break;
      __builtin_amdgcn_s_sleep(8);
      pw = __builtin_amdgcn_raw_buffer_load_b128(rp, lane * 16, 0, 16);
      asm volatile("" ::: "memory");
    }
    lds_signal<SW_P2>(U);
    asm volatile("" : "+v"(pw));
  } else {
    bars(N - 5);
  }
  {
    constexpr uint32_t img = 256 + ((NX - 3) & 3) * G0_STAGE;
    if (f) lds_wait<SW_P2, 1>(U, 1);
    gdev2(ws + WS_PG, vo, vp, img + f * 4096);
    gdev2(ws + WS_PG + 2048, vo, vp, img + f * 4096 + 2048);
    if (f == 0) scales();
    lane = lane_id();
    l15 = lane & 15;
    q = lane >> 4;
    uint32_t xc = img + (l15 + 64 * q) * 128 + (f ^ (l15 >> 1)) * 16;
    uint32_t xr = img + f * 4096 + q * 128 + ((l15 >> 1) ^ (q >> 1)) * 16 + (l15 & 1) * 8;
    int oo = (32 * f + q) * 65536 + (li & 7) * 8192 + (li >> 3) * 256 + l15 * 16;
    lds_cf2(smem + L_TB + 2048 + lane * 16, tw[0][0], tw[0][1]), lds_cf2(smem + L_TB + 3072 + lane * 16, tw[0][2], tw[0][3]);
    asm volatile("" : "+v"(xc), "+v"(xr), "+v"(oo), "+v"(tw[0][0]), "+v"(tw[0][1]), "+v"(tw[0][2]), "+v"(tw[0][3]));
    lds_wait<SW_RS, 1>(U, 2);
    u32x4 wv[4], rs;
#pragma unroll
    for (int e = 0; e < 4; ++e) {
      const int k = l15 + 64 * q + 16 * e;
      rs[e] = *(lds_u32*)(size_t)(ldsb + L_RS + ((q < 2 ? k : -k) & 127) * 4);
    }
    const int sh = __builtin_amdgcn_readfirstlane((int)*(lds_u32*)(size_t)(ldsb + L_RS + 512)) - 511;
    float sc = __builtin_amdgcn_ldexpf(1.0f, sh);
    asm volatile("" : "+v"(rs), "+v"(sc));
    wait_vm<0>();
    fsync(3);
#pragma unroll
    for (int e = 0; e < 4; ++e) wv[e] = ldsa(xc + e * 2048);
    f32x4 dr[4], di[4];
#pragma unroll
    for (int c = 0; c < 4; ++c) {
      const u32x4 x = u32x4{pk_mul_h(wv[0][c], rs[0]), pk_mul_h(wv[1][c], rs[1]), pk_mul_h(wv[2][c], rs[2]), pk_mul_h(wv[3][c], rs[3])};
      dr[c] = mfma_h(x, m16.p, Z4);
      di[c] = mfma_h(x, m16.q, Z4);
    }
#pragma unroll
    for (int c = 0; c < 4; ++c) {
      u32x4 a;
#pragma unroll
      for (int r = 0; r < 4; ++r) {
        const cf v = fin(cmul(cf{dr[c][r], di[c][r]}, tw[0][r]));
        a[r] = pk_h(v.x, v.y);
      }
      dr[c] = mfma_h(m16.p, a, Z4);
      di[c] = mfma_h(m16.q, a, Z4);
    }
    auto put = [&](auto scale) {
#pragma unroll
      for (int r = 0; r < 4; ++r) {
        u32x4 y;
#pragma unroll
        for (int c = 0; c < 4; ++c) y[c] = pk_bf(scale(dr[c][r]), scale(-di[c][r]));
        sta4(xc + r * 2048, y);
      }
    };
    if (__builtin_expect(sh >= -126 && sh <= 127, 1)) put([&](float x) { return x * sc; });
    else put([&](float x) { return __builtin_amdgcn_ldexpf(x, sh); });
    fsync(4);
    typedef __attribute__((address_space(3))) u32x2 lds_u32x2;
    u32x2 z[8];
#pragma unroll
    for (int i = 0; i < 8; ++i) z[i] = *(lds_u32x2*)(size_t)((xr ^ ((i & 3) * 32)) + i * 512);
    const __amdgpu_buffer_rsrc_t ro = __builtin_amdgcn_make_buffer_rsrc((void*)A.out, 0, 16 << 20, 0x00020000);
    lds_wait<SW_PL, 2>(U, 4);
#pragma unroll
    for (int i = 0; i < 8; ++i) {
      const uint32_t p0 = z[i][0], p1 = z[i][1];
      __builtin_amdgcn_raw_buffer_store_b128(u32x4{p0 << 16, p0 & 0xFFFF0000u, p1 << 16, p1 & 0xFFFF0000u}, ro, oo + i * 262144, 0, 2);
    }
    if (f == 0 && lane_id() < 2) st_dev((u64*)(ws + WS_F1) + bid + lane_id() * 128, 0ull);
  }
  return false;
}

__device__ __forceinline__ void publish(char* p, int lane) {
  wait_vm<0>();
  if (lane == 0) __hip_atomic_fetch_add((uint32_t*)p, 1u, __ATOMIC_RELAXED, __HIP_MEMORY_SCOPE_AGENT);
}
__device__ __forceinline__ void loader_role(const Args& A, char* smem, int wave, int lane) {
  char* const ws = A.ws;
  const Tile T = tile_map();
  const int tm = T.tm, tn = T.tn;
  lds_u32* const sw = (lds_u32*)(smem + L_SYNC);
  const uint32_t U = (uint32_t)(size_t)(sw + 1);
  const uint32_t ldsb = lds_base(smem), lds0 = ldsb + wave * 4096;
  const int prow = lane >> 3;
  {
    __builtin_amdgcn_s_setprio(0);
    const uint32_t va = (tm * 128 + wave * 32 + prow) * 2048 + ((lane & 7) ^ (prow & 3)) * 16;
    const uint32_t vb = (tn * 64 + wave * 16 + prow) * 2048 + ((lane & 7) ^ prow) * 16;
    if (T.late) {
      bar();
      const int wc = (bid_x() >> 3) & 15;
      if ((wc & 7) != 3) {
#pragma nounroll
        for (int i = 2 + wave; i < 7; i += 4) warm_r(A, smem, lane, wc, i);
      }
      stage0(A, smem, wave, lane, 0);
    }
    stage0(A, smem, wave, lane, 1);
    stage0(A, smem, wave, lane, 2);
#pragma nounroll
    for (int k = 3; k < 16; ++k) {
      const uint32_t dst = lds0 + (k & 3) * G0_STAGE;
      const int ko = ((k + k_rot()) & 15) * 64;
      asm volatile(
          "s_mov_b32 m0, %9\n\ts_waitcnt vmcnt(8)\n\ts_barrier\n\t"
          "global_load_lds_dwordx4 %0, %6\n\tglobal_load_lds_dwordx4 %1, %6 offset:1024\n\t"
          "global_load_lds_dwordx4 %2, %6 offset:2048\n\tglobal_load_lds_dwordx4 %3, %6 offset:3072\n\t"
          "s_mov_b32 m0, %10\n\ts_nop 0\n\t"
          "global_load_lds_dwordx4 %4, %7\n\tglobal_load_lds_dwordx4 %5, %7 offset:1024\n\t"
          "global_load_lds_dwordx4 %4, %8 offset:2048\n\tglobal_load_lds_dwordx4 %5, %8 offset:3072"
          ::"v"(va), "v"(va + 15360), "v"((va ^ 64) + 2 * 15360), "v"((va ^ 64) + 3 * 15360), "v"(vb), "v"(vb + 15360),
            "s"(A.xB + ko), "s"(A.Bre + ko), "s"(A.Bim + ko - 1024), "s"(dst), "s"(dst + 16384)
          : "memory");
    }
    wait_vm<8>();
    bar();
    wait_vm<0>();
    lds_signal<SW_LD>(U);
  }
  __builtin_amdgcn_s_setprio(2);
  {
    const int nseg = 2 * tm - 8 * wave;
    cf G = cf{0.f, 0.f};
    lds_wait<SW_RS, 2>(U, 1);
    if (nseg > 0) {
      const __amdgpu_buffer_rsrc_t ra = __builtin_amdgcn_make_buffer_rsrc((void*)(ws + WS_AGG), 0, 1 << 20, 0x00020000);
      int o[8];
#pragma unroll
      for (int k = 0; k < 8; ++k) {
        o[k] = ((nseg - 1 - k) * 16 + tn) * 2048 + agg_off(lane);
        asm volatile("" : "+v"(o[k]));
      }
      u32x4 u[8];
      cf l64 = reinterpret_cast<const cf*>(smem + L_PART)[192 + lane], l128 = reinterpret_cast<const cf*>(smem + L_PART)[384 + lane];
      cf lw = reinterpret_cast<const cf*>(smem + L_GRP)[wave * 64 + lane];
      asm volatile("" : "+v"(l64), "+v"(l128), "+v"(lw));
      lds_wait<SW_PUB, 1>(U, 4);
      __builtin_amdgcn_s_sleep(10);
#pragma nounroll
      for (unsigned spins = 0; spins < SPIN_MAX; ++spins) {
        uint32_t n = 0;
#pragma unroll
        for (int k = 0; k < 8; ++k) u[k] = __builtin_amdgcn_raw_buffer_load_b128(ra, o[k], 0, 16);
#pragma unroll
        for (int k = 0; k < 8; ++k) n += u[k][1] + u[k][3];
        if (__builtin_amdgcn_ballot_w64(n != 2u * (nseg < 8 ? nseg : 8)) == 0) break;
        asm volatile("" ::: "memory");
      }
#pragma unroll
      for (int k = 6; k >= 0; k -= 2)
        G = cmad(G, l128, cmad(cf{u2f(u[k + 1][0]), u2f(u[k + 1][2])}, l64, cf{u2f(u[k][0]), u2f(u[k][2])}));
      G = cmul(G, lw);
    }
    reinterpret_cast<cf*>(smem + L_GRP)[wave * 64 + lane] = G;
    wait_lgkm0();
    lds_signal<SW_GRP>(U);
  }
  if (!T.late) {
    lds_wait<SW_GRP, 2>(U, 4);
    lds_wait<SW_G0E, 2>(U, 4);
    stage_c(A, wave, lane, 0);
    lds_wait<SW_HS, 2>(U, 4);
    stage_c(A, wave, lane, 1);
    stage_c(A, wave, lane, 2);
    lds_wait<SW_HD, 2>(U, 4);
    warm1(smem, (wave & 1) ? A.Cim : A.Cre, (T.nt * 128 + (tm >> 2) * 32 + (lane & 31)) * 2048 + (10 + (wave & 2) + (lane >> 5)) * 128);
  }
  const char* ab = ws + WS_PG + tm * (2 << 20) + (wave >> 1) * (32 * 16384) + half_of(lds_peek<SW_HALF>(U), tm, wave >> 1) + (wave & 1) * 4096;
  uint32_t vh = lane * 16, vc = g1_vc(wave, lane);
  asm volatile("" : "+v"(vh), "+v"(vc));
  lds_wait<SW_G1, 1>(U, 1);
  __builtin_amdgcn_s_setprio(0);
  const int N = T.late ? 32 - NX : NX;
#pragma nounroll
  for (int k = T.late ? 2 : 0; k < 3; ++k) {
    const char* pa = ab + g1_kt(T.late, k) * 16384;
    glds2_dev(pa, vh, lds0 + (k + 1) * G0_STAGE);
    glds2_dev(pa + 2048, vh, lds0 + (k + 1) * G0_STAGE + 2048);
  }
  wait_vm<4>();
#pragma nounroll
  for (int k = 3; k < N; ++k) {
    const uint32_t dst = lds0 + ((k + 1) & 3) * G0_STAGE;
    const int kt = g1_kt(T.late, k);
    asm volatile(
        "s_mov_b32 m0, %5\n\ts_waitcnt vmcnt(8)\n\ts_barrier\n\t"
        "global_load_lds_dwordx4 %2, %4 sc1\n\tglobal_load_lds_dwordx4 %2, %4 offset:1024 sc1\n\t"
        "global_load_lds_dwordx4 %2, %4 offset:2048 sc1\n\tglobal_load_lds_dwordx4 %2, %4 offset:3072 sc1\n\t"
        "s_mov_b32 m0, %6\n\ts_nop 0\n\t"
        "global_load_lds_dwordx4 %0, %3\n\tglobal_load_lds_dwordx4 %1, %3 offset:1024\n\t"
        "global_load_lds_dwordx4 %0, %3 offset:2048\n\tglobal_load_lds_dwordx4 %1, %3 offset:3072"
        ::"v"(vc), "v"(vc + 65536 - 1024), "v"(vh), "s"(((kt & 1) ? A.Cim : A.Cre) + (kt >> 1) * 64), "s"(ab + kt * 16384), "s"(dst),
          "s"(dst + 16384)
        : "memory");
  }
  wait_vm<8>();
  bar();
  wait_vm<0>();
  lds_signal<SW_LD>(U);
  if (T.late) {
    const char* ps = ps_of(ws, lds_peek<SW_HALF>(U), tm, T.nt, wave);
    lds_wait<SW_PX, 1>(U, 1);
#pragma nounroll
    for (int i = 0; i < 4; ++i) glds2_dev(ps + i * 2048, lane * 16, ldsb + ((29 - NX) & 3) * G0_STAGE + wave * 8192 + i * 2048);
    wait_vm<0>();
    lds_signal<SW_PL>(U);
  }
}

#define W(n) "s_waitcnt lgkmcnt(" #n ")\n\t"
#define M(c, a, b) "v_mfma_f32_16x16x32_bf16 v[" #c ":" #c "+3], v[" #a ":" #a "+3], v[" #b ":" #b "+3], v[" #c ":" #c "+3]\n\t"
#define RB(d, p, o) "ds_read_b128 v[" #d ":" #d "+3], v" #p " offset:" #o "\n\t"
#define RT(d, p, o) "ds_read_b64_tr_b16 v[" #d ":" #d "+1], v" #p " offset:" #o "\n\t"
#define AD(p) "v_add_u32 v" #p ", %[adv], v" #p "\n\t"
#define SA "s_add_i32 %[k], %[k], 1\n\ts_and_b32 %[adv], %[k], 3\n\t"
#define SB "s_cmp_eq_u32 %[adv], 0\n\ts_cselect_b32 %[adv], %[m3], 0x8000\n\t"
#define F(d, p, l) "v_fmac_f32 v[" #d "], v[" #p "], v" #l "\n\t"
#define XA(d, p) F(d, p + 4, 122) F(d + 4, p + 4, 104) F(d + 8, p + 12, 123) F(d + 12, p + 12, 106)
#define XB(d, p) F(d, p, 104) F(d + 4, p, 105) F(d + 8, p + 8, 106) F(d + 12, p + 8, 107)
#define LA0 "v_mbcnt_lo_u32_b32 v108, -1, 0\n\tv_mbcnt_hi_u32_b32 v108, -1, v108\n\t"
#define LA1 "v_and_b32 v108, 15, v108\n\tv_lshl_add_u32 v108, v108, 3, %[pp]\n\t"
#define LAM "ds_read_b64 v[104:105], v108 offset:%[lo]\n\tds_read_b64 v[106:107], v108 offset:%[lo]+128\n\ts_setprio 3\n\t"
#define L16 "ds_read2_b64 v[64:67], v108 offset0:64 offset1:80\n\tds_read2_b64 v[68:71], v108 offset0:128 offset1:144\n\t"
#define NEG "v_xor_b32 v122, 0x80000000, v105\n\tv_xor_b32 v123, 0x80000000, v107\n\t"
#define SIG "v_mov_b32 v109, 4\n\ts_mov_b64 exec, 1\n\tds_add_u32 v109, v109 offset:%[e]\n\ts_mov_b64 exec, -1\n\t"
#define STEP0(C0, CN, H4, H9, H19, H30) \
  W(7) M(0, 64, C0) RB(72, 120, 512) W(7) M(0, 68, C0 + 4) RB(76, 121, 512) \
  W(7) M(4, 64, 80) W(6) M(4, 68, 84) \
  W(5) M(8, 64, 88) SA H4 W(4) M(8, 68, 92) SB \
  W(3) M(12, 64, 96) RB(64, 120, 1024) W(3) M(12, 68, 100) RB(68, 121, 1024) \
  W(3) M(16, 72, C0) W(2) M(16, 76, C0 + 4) AD(122) H9 \
  M(20, 72, 80) RB(CN, 122, 0) M(20, 76, 84) AD(123) \
  M(24, 72, 88) RB(CN + 4, 123, 0) M(24, 76, 92) \
  M(28, 72, 96) RB(72, 120, 1536) M(28, 76, 100) RB(76, 121, 1536) \
  W(5) M(32, 64, C0) W(4) M(32, 68, C0 + 4) \
  M(36, 64, 80) M(36, 68, 84) H19 \
  M(40, 64, 88) AD(120) M(40, 68, 92) AD(121) \
  M(44, 64, 96) RB(64, 120, 0) M(44, 68, 100) RB(68, 121, 0) \
  W(3) M(52, 72, 80) RB(80, 122, 2048) W(3) M(52, 76, 84) RB(84, 123, 2048) \
  M(56, 72, 88) RB(88, 122, 4096) M(56, 76, 92) RB(92, 123, 4096) \
  M(60, 72, 96) RB(96, 122, 6144) M(60, 76, 100) RB(100, 123, 6144) \
  M(48, 72, C0) H30 M(48, 76, C0 + 4)
#define HEAD0(C0) \
  RB(C0, 122, 0) RB(C0 + 4, 123, 0) RB(64, 120, 0) RB(68, 121, 0) RB(80, 122, 2048) RB(84, 123, 2048) RB(88, 122, 4096) RB(92, 123, 4096) RB(96, 122, 6144) RB(100, 123, 6144)
#define STEP1(C0, CN, H4, H9, H19, H30) \
  W(8) M(0, 64, C0) RT(72, 120, 2048) W(6) M(0, 68, C0 + 4) RT(74, 121, 2048) \
  M(4, 64, 80) RT(76, 120, 2560) W(7) M(4, 68, 84) RT(78, 121, 2560) \
  W(7) M(8, 64, 88) SA H4 W(6) M(8, 68, 92) SB \
  W(5) M(12, 64, 96) RT(64, 120, 4096) W(5) M(12, 68, 100) RT(66, 121, 4096) \
  W(4) M(16, 72, C0) RT(68, 120, 4608) W(3) M(16, 76, C0 + 4) RT(70, 121, 4608) \
  M(20, 72, 80) AD(122) H9 M(20, 76, 84) RB(CN, 122, 0) \
  M(24, 72, 88) AD(123) M(24, 76, 92) RB(CN + 4, 123, 0) \
  M(28, 72, 96) RT(72, 120, 6144) M(28, 76, 100) RT(74, 121, 6144) \
  W(6) M(32, 64, C0) RT(76, 120, 6656) W(5) M(32, 68, C0 + 4) RT(78, 121, 6656) \
  M(36, 64, 80) M(36, 68, 84) H19 \
  M(40, 64, 88) AD(120) M(40, 68, 92) AD(121) \
  M(44, 64, 96) RT(64, 120, 0) M(44, 68, 100) RT(66, 121, 0) \
  W(4) M(52, 72, 80) RT(68, 120, 512) RB(80, 122, 2048) W(4) M(52, 76, 84) RT(70, 121, 512) RB(84, 123, 2048) \
  M(56, 72, 88) RB(88, 122, 4096) M(56, 76, 92) RB(92, 123, 4096) \
  M(60, 72, 96) RB(96, 122, 6144) M(60, 76, 100) RB(100, 123, 6144) \
  M(48, 72, C0) H30 M(48, 76, C0 + 4)
#define HEAD1(C0) \
  RB(C0, 122, 0) RB(C0 + 4, 123, 0) RT(64, 120, 0) RT(66, 121, 0) RT(68, 120, 512) RB(80, 122, 2048) RT(70, 121, 512) RB(84, 123, 2048) RB(88, 122, 4096) RB(92, 123, 4096) RB(96, 122, 6144) RB(100, 123, 6144)
#define ROW1(C0) \
  W(8) M(0, 64, C0) RT(72, 120, 2048) W(6) M(0, 68, C0 + 4) RT(74, 121, 2048) \
  M(4, 64, 80) RT(76, 120, 2560) W(7) M(4, 68, 84) RT(78, 121, 2560) \
  W(7) M(8, 64, 88) W(6) M(8, 68, 92) \
  W(5) M(12, 64, 96) W(4) M(12, 68, 100) \
  W(0)
#define TAIL0 \
  W(7) M(0, 64, 112) RB(72, 120, 512) LA0 W(7) M(0, 68, 116) RB(76, 121, 512) LA1 \
  W(7) M(4, 64, 80) LAM W(8) M(4, 68, 84) \
  W(7) M(8, 64, 88) W(6) M(8, 68, 92) \
  W(5) M(12, 64, 96) RB(64, 120, 1024) W(5) M(12, 68, 100) RB(68, 121, 1024) \
  W(5) M(16, 72, 112) W(2) NEG M(16, 76, 116) \
  M(20, 72, 80) XA(1, 0) M(20, 76, 84) XB(1, 0) \
  M(24, 72, 88) XA(2, 1) M(24, 76, 92) XB(2, 1) \
  M(28, 72, 96) RB(72, 120, 1536) XA(3, 2) M(28, 76, 100) RB(76, 121, 1536) XB(3, 2) \
  W(3) M(32, 64, 112) W(2) M(32, 68, 116) \
  M(36, 64, 80) XA(16, 3) M(36, 68, 84) XB(16, 3) \
  M(40, 64, 88) XA(17, 16) M(40, 68, 92) XB(17, 16) \
  M(44, 64, 96) XA(18, 17) M(44, 68, 100) XB(18, 17) \
  W(1) M(48, 72, 112) XA(19, 18) W(0) M(48, 76, 116) SIG XB(19, 18) \
  M(52, 72, 80) L16 XA(32, 19) M(52, 76, 84) XB(32, 19) \
  M(56, 72, 88) XA(33, 32) M(56, 76, 92) XB(33, 32) \
  M(60, 72, 96) XA(34, 33) M(60, 76, 100) XB(34, 33) \
  XA(35, 34) XB(35, 34) "s_nop 3\n\t" XA(48, 35) XB(48, 35) \
  XA(49, 48) XB(49, 48) XA(50, 49) XB(50, 49) \
  XA(51, 50) XB(51, 50) W(0)
#define V2(op, d, a, b) op " v[" #d "], " a ", v[" #b "]\n\t"
#define CV(d, a) "v_cvt_pk_bf16_f32 v[" #d "], v[" #a "], v[" #a "+4]\n\t"
#define ST(d, inc, pol) "buffer_store_dwordx4 v[" #d ":" #d "+3], v123, %[rs], %[sf] offen " pol "\n\ts_add_u32 %[sf], %[sf], " #inc "\n\t"
#define C4(d, a) CV(d, a) CV(d + 1, a + 8) CV(d + 2, a + 1) CV(d + 3, a + 9)
#define SP(d) ST(d, 1024, "sc1")
#define UN(d, p) V2("v_lshlrev_b32", d, "16", p) V2("v_and_b32", d + 1, "0xffff0000", p) V2("v_lshlrev_b32", d + 2, "16", p + 1) V2("v_and_b32", d + 3, "0xffff0000", p + 1)
#define A1(d, a) "v_add_f32 v[" #d "], v[" #a "], v[" #d "]\n\t"
#define AS(d, a, inc) A1(d, a) A1(d + 1, a + 4) A1(d + 2, a + 8) A1(d + 3, a + 12) ST(d, inc, "nt")
#define LE1 "v_lshlrev_b32 v123, 4, v108\n\t"
#define LL1 "v_lshl_add_u32 v122, v108, 4, %[pp]\n\tv_and_b32 v123, 15, v108\n\t"
#define LL2 "v_lshrrev_b32 v108, 4, v108\n\tv_lshlrev_b32 v108, 15, v108\n\tv_lshl_add_u32 v123, v123, 4, v108\n\t"
#define PLW "v_mov_b32 v104, 0\n6:\n\tds_read_b32 v105, v104 offset:%[e]\n\t" W(0) "v_readfirstlane_b32 %[t], v105\n\ts_cmp_lt_u32 %[t], 16\n\ts_cbranch_scc1 6b\n\t"
#define TAIL1E \
  RT(64, 120, 4096) RT(66, 121, 4096) RT(68, 120, 4608) RT(70, 121, 4608) M(16, 72, 112) LA0 LE1 M(16, 76, 116) \
  M(20, 72, 80) M(20, 76, 84) \
  M(24, 72, 88) M(24, 76, 92) C4(104, 0) SP(104) \
  M(28, 72, 96) RT(72, 120, 6144) M(28, 76, 100) RT(74, 121, 6144) C4(108, 2) SP(108) \
  W(4) M(32, 64, 112) RT(76, 120, 6656) W(3) M(32, 68, 116) RT(78, 121, 6656) \
  M(36, 64, 80) M(36, 68, 84) \
  M(40, 64, 88) M(40, 68, 92) C4(0, 16) SP(0) \
  M(44, 64, 96) M(44, 68, 100) C4(4, 18) SP(4) \
  W(2) M(48, 72, 112) W(0) M(48, 76, 116) \
  M(52, 72, 80) M(52, 76, 84) \
  M(56, 72, 88) M(56, 76, 92) C4(8, 32) SP(8) \
  M(60, 72, 96) M(60, 76, 100) C4(12, 34) SP(12) \
  "s_nop 7\n\t" C4(16, 48) C4(20, 50) \
  SP(16) SP(20)
#define TAIL1L \
  RT(64, 120, 4096) RT(66, 121, 4096) RT(68, 120, 4608) RT(70, 121, 4608) M(16, 72, 112) LA0 LL1 M(16, 76, 116) LL2 \
  M(20, 72, 80) PLW RB(104, 122, 0) M(20, 76, 84) \
  M(24, 72, 88) W(0) UN(108, 104) M(24, 76, 92) AS(108, 0, 8192) \
  M(28, 72, 96) RT(72, 120, 6144) RB(108, 122, 1024) UN(104, 106) M(28, 76, 100) RT(74, 121, 6144) AS(104, 1, 8192) \
  M(32, 64, 112) RT(76, 120, 6656) W(2) UN(104, 108) M(32, 68, 116) RT(78, 121, 6656) AS(104, 2, 8192) \
  M(36, 64, 80) RB(104, 122, 2048) UN(108, 110) M(36, 68, 84) AS(108, 3, 106496) \
  M(40, 64, 88) RB(108, 122, 3072) M(40, 68, 92) W(1) UN(0, 104) \
  M(44, 64, 96) AS(0, 16, 8192) M(44, 68, 100) UN(4, 106) \
  M(48, 72, 112) AS(4, 17, 8192) RB(104, 122, 4096) M(48, 76, 116) W(1) UN(8, 108) \
  M(52, 72, 80) AS(8, 18, 8192) M(52, 76, 84) UN(12, 110) \
  M(56, 72, 88) AS(12, 19, 106496) RB(108, 122, 5120) M(56, 76, 92) W(1) UN(16, 104) \
  M(60, 72, 96) AS(16, 32, 8192) M(60, 76, 100) UN(20, 106) \
  AS(20, 33, 8192) RB(104, 122, 6144) W(1) UN(24, 108) AS(24, 34, 8192) UN(28, 110) \
  AS(28, 35, 106496) RB(108, 122, 7168) "s_nop 7\n\t" W(1) UN(32, 104) AS(32, 48, 8192) UN(36, 106) \
  AS(36, 49, 8192) W(0) UN(40, 108) AS(40, 50, 8192) UN(44, 110) \
  AS(44, 51, 106496)
#define ASK "v_mov_b32 v112, 0\n\tds_read_b32 v112, v112 offset:%[o]\n\t"
#define CHK "v_readfirstlane_b32 %[t], v112\n\ts_cmp_lt_u32 %[t], %[cnt]\n\t"
#define LOOP(X)                                                                                                           \
  "1:\n\t" STEP##X(104, 112, , , , "s_barrier\n\t") "5:\n\t"                                                                \
  STEP##X(112, 104, , , "s_cmp_lg_u32 %[k], %[k1]\n\t", "s_cbranch_scc0 2f\n\ts_barrier\n2:\n\t") "s_cbranch_scc1 1b\n\t" \
  STEP##X(104, 112, ASK, CHK "s_cbranch_scc0 4f\n3:\n\t" ASK W(0) CHK "s_cbranch_scc1 3b\n4:\n\t", , )
#define ACC \
  "+{v[0:3]}"(acc[0][0]), "+{v[4:7]}"(acc[0][1]), "+{v[8:11]}"(acc[0][2]), "+{v[12:15]}"(acc[0][3]), \
  "+{v[16:19]}"(acc[1][0]), "+{v[20:23]}"(acc[1][1]), "+{v[24:27]}"(acc[1][2]), "+{v[28:31]}"(acc[1][3]), \
  "+{v[32:35]}"(acc[2][0]), "+{v[36:39]}"(acc[2][1]), "+{v[40:43]}"(acc[2][2]), "+{v[44:47]}"(acc[2][3]), \
  "+{v[48:51]}"(acc[3][0]), "+{v[52:55]}"(acc[3][1]), "+{v[56:59]}"(acc[3][2]), "+{v[60:63]}"(acc[3][3]), \
  "+{v120}"(pa0), "+{v121}"(pa1), "+{v122}"(pb0), "+{v123}"(pb1)
#define CLOB \
  "scc", "memory", "v64", "v65", "v66", "v67", "v68", "v69", "v70", "v71", "v72", "v73", "v74", "v75", "v76", "v77", "v78", "v79", \
  "v80", "v81", "v82", "v83", "v84", "v85", "v86", "v87", "v88", "v89", "v90", "v91", "v92", "v93", "v94", "v95", "v96", "v97", \
  "v98", "v99", "v100", "v101", "v102", "v103", "v104", "v105", "v106", "v107", "v108", "v109", "v110", "v111", "v112", "v113", \
  "v114", "v115", "v116", "v117", "v118", "v119"
__device__ __forceinline__ void run0(f32x4 (&acc)[4][4], uint32_t pa0, uint32_t pb0, uint32_t pp, u32x4& lam, u32x4& l16, u32x4& l32) {
  uint32_t pa1 = pa0 ^ 64, pb1 = pb0 ^ 64, t;
  int k = 0, adv;
  asm volatile("" : ACC);
  bar_lds();
  asm volatile(HEAD0(104) LOOP(0) TAIL0
               : ACC, [k] "+s"(k), [adv] "=&s"(adv), [t] "=&s"(t)
               : [k1] "n"(14), [m3] "s"(-3 * G0_STAGE), [cnt] "n"(16), [o] "n"(SW_LD * 4), [e] "n"(SW_G0E * 4 - 4), [pp] "s"(pp), [lo] "n"(L_PRM - L_PART)
               : CLOB);
  asm volatile("" : "={v[104:107]}"(lam), "={v[64:67]}"(l16), "={v[68:71]}"(l32));
  __builtin_amdgcn_sched_barrier(0);
}
__device__ __forceinline__ void run1(f32x4 (&acc)[4][4], uint32_t pa0, uint32_t pb0, int n, uint32_t pp, u32x4 rs, uint32_t sf) {
  uint32_t pa1 = pa0 ^ 64, pb1 = pb0 ^ 64, t;
  int k = 1, adv;
  asm volatile("" : ACC);
  bar_lds();
#define REST \
  LOOP(1) ROW1(112) "s_cmp_lg_u32 %[pp], 0\n\ts_cbranch_scc1 7f\n\t" TAIL1E "s_branch 8f\n7:\n\t" TAIL1L "8:" \
  : ACC, [k] "+s"(k), [adv] "=&s"(adv), [t] "=&s"(t), [sf] "+s"(sf) \
  : [k1] "s"(n - 1), [m3] "s"(-3 * G0_STAGE), [cnt] "n"(32), [o] "n"(SW_LD * 4), [e] "n"(SW_PL * 4), [pp] "s"(pp), [rs] "s"(rs) \
  : CLOB
  if (NX & 1) asm volatile(HEAD1(112) "s_branch 5f\n\t" REST);
  else asm volatile(HEAD1(104) REST);
}
#undef W
#undef M

__device__ __forceinline__ void mfma_role(const Args& A, char* smem, int wave, int lane) {
  char* const ws = A.ws;
  const Tile T = tile_map();
  const int tm = T.tm, tn = T.tn;
  lds_u32* const sw = (lds_u32*)(smem + L_SYNC);
  const uint32_t U = (uint32_t)(size_t)(sw + 1);
  const int wr = wave >> 1, wc = wave & 1;
  int fr = lane & 15, fq = lane >> 4;
  uint32_t hw;
  if (T.late) {
    uint32_t n[2] = {0, 0};
    auto probe = [&](int i) {
      const int h = (i ^ (i >> 1) ^ wave) & 1;
      u64 t0, t1;
      uint32_t v;
      asm volatile("s_memtime %0\n\ts_waitcnt lgkmcnt(0)\n\ts_load_dword %2, %3, 0\n\ts_waitcnt lgkmcnt(0)\n\ts_memtime %1\n\ts_waitcnt lgkmcnt(0)"
                   : "=&s"(t0), "=&s"(t1), "=&s"(v)
                   : "s"(ws + WS_PG + tm * (2 << 20) + (2 << 20) - 32768 + (T.nt >> 2) * 16384 + h * 8192 + (((T.nt & 3) * 3 + wave) * 5 + i) * 128)
                   : "memory");
      const uint32_t d = (uint32_t)(t1 - t0), g = d < 4096u ? 4096u - d : 0u;
      n[0] = !h && g > n[0] ? g : n[0], n[1] = h && g > n[1] ? g : n[1];
    };
    const u64 t0 = __builtin_amdgcn_s_memrealtime();
    bar_lds();
    if (wave < 3) {
#pragma nounroll
      for (int i = 0; i < 5 && __builtin_amdgcn_s_memrealtime() - t0 < 200; ++i) probe(i);
      if (lane < 2) __hip_atomic_fetch_max(sw + SW_N0 + lane, lane ? n[1] : n[0], __ATOMIC_RELAXED, __HIP_MEMORY_SCOPE_WORKGROUP);
    }
  }
  if (wave == 3) {
    wait_vm<0>();
    const u16* raw = reinterpret_cast<const u16*>(smem + L_PRM + 512);
    uint32_t pr = raw[lane], pi = raw[64 + lane], pd = raw[128 + lane];
    asm volatile("" : "+v"(pr), "+v"(pi), "+v"(pd)::"memory");
    auto expf_ = [](float x) {
      const float t = x * 1.44269504088896341f;
      const float r = __builtin_fmaf(x, 1.44269504088896341f, -t) + x * 1.92596299112661746e-8f;
      const float e = __builtin_amdgcn_exp2f(t);
      return __builtin_fmaf(e * r, 0.693147180559945309f, e);
    };
    const float lr = u2f(pr << 16), li = u2f(pi << 16);
    const float dt = rbf(expf_(u2f(pd << 16)));
    const float zr = dt * lr, zi = dt * li;
    const float e = expf_(zr);
    const float rh = zi * 0.159154936671257019f;
    const float rf = (rh - __builtin_rintf(rh)) + __builtin_fmaf(zi, 6.42063832653e-9f, __builtin_fmaf(zi, 0.159154936671257019f, -rh));
    const cf lam = cf{e * __builtin_amdgcn_cosf(rf), e * __builtin_amdgcn_sinf(rf)};
    const float nr = lam.x - 1.0f, ni = lam.y;
    const float den = 2.0f / (lr * lr + li * li);
    cf* prm = reinterpret_cast<cf*>(smem + L_PRM);
    prm[lane] = lam;
    prm[64 + lane] = cf{(nr * lr + ni * li) * den, (ni * lr - nr * li) * den};
    cf lp[10];
    lp[0] = cmul(lam, lam);
#pragma unroll
    for (int k = 1; k < 10; ++k) lp[k] = cmul(lp[k - 1], lp[k - 1]);
    cf* pt = reinterpret_cast<cf*>(smem + L_PART) + lane;
    pt[64] = lp[3], pt[128] = lp[4], pt[192] = lp[5], pt[256] = lp[0], pt[320] = lp[1], pt[384] = lp[6];
    pt = reinterpret_cast<cf*>(smem + L_GRP) + lane;
    pt[0] = cf{1.f, 0.f}, pt[64] = lp[8], pt[128] = lp[9], pt[192] = cmul(lp[8], lp[9]);
  }
  __builtin_amdgcn_s_setprio(1);
  {
    f32x4 acc[4][4];
#pragma unroll
    for (int m = 0; m < 4; ++m)
#pragma unroll
      for (int n = 0; n < 4; ++n) acc[m][n] = Z4;
    const cf* prm = reinterpret_cast<const cf*>(smem + L_PRM) + wc * 32;
    cf* part = reinterpret_cast<cf*>(smem + L_PART) + wc * 32;
    cf lam[2], l16[2], l32[2], s[2][16];
    const uint64_t ag = (uint64_t)(ws + WS_AGG) + ((2 * tm + wr) * 16 + tn) * 2048 + wc * 640;
    u32x4 ra = u32x4{(uint32_t)ag, (uint32_t)(ag >> 32) & 0xFFFFu, wc ? 512u : 640u, 0x00020000u};
    {
      u32x4 t0, t1, t2;
      asm volatile("" : "+s"(ra));
      run0(acc, lds_base(smem) + (wr * 64 + (fr >> 2) * 16 + (fr & 3)) * 128 + (fq ^ (fr & 7)) * 16,
           lds_base(smem) + (128 + wc * 64 + fr) * 128 + (fq ^ (fr & 7)) * 16, lds_base(smem) + L_PART + wc * 256, t0, t1, t2);
#pragma unroll
      for (int jp = 0; jp < 2; ++jp) {
        lam[jp] = cf{u2f(t0[2 * jp]), u2f(t0[2 * jp + 1])};
        l16[jp] = cf{u2f(t1[2 * jp]), u2f(t1[2 * jp + 1])};
        l32[jp] = cf{u2f(t2[2 * jp]), u2f(t2[2 * jp + 1])};
      }
      lane = lane_id();
      fr = lane & 15;
      fq = lane >> 4;
#pragma unroll
      for (int i = 0; i < 32; ++i) s[i >> 4][i & 15] = cf{acc[(i >> 2) & 3][2 * (i >> 4)][i & 3], acc[(i >> 2) & 3][2 * (i >> 4) + 1][i & 3]};
    }
    const uint64_t m1 = 0xFFFF0000FFFF0000ull, m2 = 0xFFFFFFFF00000000ull;
    cf pe[2], a01[2], wg[2];
#pragma unroll
    for (int jp = 0; jp < 2; ++jp) {
      const u32x2 ex = __builtin_amdgcn_permlane16_swap(f2u(s[jp][15].x), f2u(s[jp][15].x), false, false);
      const u32x2 ey = __builtin_amdgcn_permlane16_swap(f2u(s[jp][15].y), f2u(s[jp][15].y), false, false);
      pe[jp] = cf{u2f(ex[0]), u2f(ey[0])};
      const cf ap = cmad(pe[jp], l16[jp], cf{u2f(ex[1]), u2f(ey[1])});
      const u32x2 hx = __builtin_amdgcn_permlane32_swap(f2u(ap.x), f2u(ap.x), false, false);
      const u32x2 hy = __builtin_amdgcn_permlane32_swap(f2u(ap.y), f2u(ap.y), false, false);
      a01[jp] = cf{u2f(hx[0]), u2f(hy[0])};
      wg[jp] = cmad(a01[jp], l32[jp], cf{u2f(hx[1]), u2f(hy[1])});
    }
    {
      const cf ws_ = sel_c(m1, wg[1], wg[0]);
      asm volatile("buffer_store_dwordx4 %0, %1, %2, 0 offen sc1\n\ts_nop 1"
                   ::"v"(u32x4{f2u(ws_.x), 1u, f2u(ws_.y), 1u}), "v"(wc ? lane * 16 : agg_off(lane)), "s"(ra) : "memory");
      if (wr == 0) {
        part[lane & 31] = ws_;
        wait_lgkm0();
      }
      lds_signal<SW_PUB>(U);
      lds_wait<SW_PUB, 0>(U, 4);
    }
    lds_wait<SW_RS, 1>(U, 1);
    lds_wait<SW_RS, 1>(U, 1);
    hw = lds_peek<SW_HALF>(U);
    __builtin_amdgcn_s_setprio(0);
    cf l2[2], l3[2], l4[2], c[2], pw[2];
#pragma unroll
    for (int jp = 0; jp < 2; ++jp) {
      const cf cof = prm[64 + jp * 16 + fr];
      l2[jp] = part[256 + jp * 16 + fr];
      l4[jp] = part[320 + jp * 16 + fr];
      const cf p1 = sel_c(m1, l16[jp], cf{1.f, 0.f});
      cf cc = sel_c(m1, pe[jp], cf{0.f, 0.f});
      cc = sel_c(m2, cmad(p1, a01[jp], cc), cc);
      cf p = sel_c(m2, cmul(p1, l32[jp]), p1);
      if (wr) {
        cc = cmad(p, part[jp * 16 + fr], cc);
        p = cmul(p, part[192 + jp * 16 + fr]);
      }
      pw[jp] = cmul(p, cof);
      c[jp] = cmul(cc, cof);
      l3[jp] = cmul(l2[jp], lam[jp]);
      asm volatile("" : "+v"(pw[jp]), "+v"(c[jp]), "+v"(l3[jp]), "+v"(l4[jp]));
#pragma unroll
      for (int i = 0; i < 16; ++i) {
        s[jp][i] = cmul(s[jp][i], cof);
        asm volatile("" : "+v"(s[jp][i]));
      }
    }
    const uint64_t hs = (uint64_t)(ws + WS_PG) + tm * (2 << 20) + (wr * 32 + tn * 2) * 16384 + half_of(hw, tm, wr);
    u32x4 rs = u32x4{(uint32_t)hs, (uint32_t)(hs >> 32) & 0xFFFFu, 32768u, 0x00020000u};
    uint32_t hb[2], ho[2], ga = lds_base(smem) + L_GRP + (wc * 32 + fr) * 8;
#pragma unroll
    for (int h = 0; h < 2; ++h) hb[h] = (fq * 2048 + (wc * 32 + fr) * 16) ^ (h * 64), ho[h] = hb[h] + lds_base(smem) + G0_STAGE + wr * 8192;
    asm volatile("" : "+s"(rs), "+v"(hb[0]), "+v"(hb[1]), "+v"(ho[0]), "+v"(ho[1]), "+v"(ga));
    __builtin_amdgcn_s_setprio(3);
    lds_wait<SW_GRP, 1>(U, 4);
    typedef __attribute__((address_space(3))) cf lds_cf;
    cf t[2];
#pragma unroll
    for (int jp = 0; jp < 2; ++jp) {
      const lds_cf* g = (const lds_cf*)(size_t)ga + jp * 16;
      t[jp] = cmad(pw[jp], (g[192] + g[128]) + (g[64] + g[0]), c[jp]);
    }
    asm volatile("" : "+v"(t[0]), "+v"(t[1]));
#pragma unroll
    for (int jh = 0; jh < 4; ++jh) {
      const int jp = jh >> 1, h = jh & 1;
#pragma unroll
      for (int k = 2 * h; k < 2 * h + 2; ++k) {
        s[jp][4 * k] = cmad(t[jp], lam[jp], s[jp][4 * k]);
        s[jp][4 * k + 1] = cmad(t[jp], l2[jp], s[jp][4 * k + 1]);
        s[jp][4 * k + 2] = cmad(t[jp], l3[jp], s[jp][4 * k + 2]);
        t[jp] = cmul(t[jp], l4[jp]);
        s[jp][4 * k + 3] = s[jp][4 * k + 3] + t[jp];
      }
      u32x4 re, im;
#pragma unroll
      for (int i = 0; i < 4; ++i) {
        re[i] = pk_bf(s[jp][8 * h + 2 * i].x, s[jp][8 * h + 2 * i + 1].x);
        im[i] = pk_bf(-s[jp][8 * h + 2 * i].y, -s[jp][8 * h + 2 * i + 1].y);
      }
      asm volatile("buffer_store_dwordx4 %0, %2, %3, 0 offen offset:%4 sc1\n\tbuffer_store_dwordx4 %1, %2, %3, %5 offen offset:%4 sc1\n\ts_nop 1"
                   ::"v"(re), "v"(im), "v"(hb[h]), "s"(rs), "n"(jp * 256 + h * 1024), "s"(16384) : "memory");
      if (T.late) sta4(ho[h] + jp * 256 + h * 1024, re), sta4(ho[h] + jp * 256 + h * 1024 + G0_STAGE, im);
      __builtin_amdgcn_sched_barrier(0);
    }
  }
  lds_signal<SW_HS>(U);
  if (T.late) {
    __builtin_amdgcn_s_setprio(0);
    wait_lgkm0();
    bar();
    bar();
    publish(counter(ws, 4 * tm + 2), lane);
    bar();
    bar();
  } else {
    publish(counter(ws, 4 * tm + (T.nt >> 2)), lane);
    lds_signal<SW_HD>(U);
    if (wave == 0) poll_count<SW_G1>(counter(ws, 4 * tm + 1), 16, U);
  }
}

__device__ __forceinline__ void gemm1(const Args& A, char* smem, int wave, int lane) {
  char* const ws = A.ws;
  const Tile T = tile_map();
  const int tm = T.tm, nt = T.nt;
  const uint32_t U = 4;
  const int wr = wave >> 1, wc = wave & 1, fr = lane & 15, fq = lane >> 4;
  f32x4 acc[4][4];
  uint32_t hw;
  if (T.late) {
    const u32x2* px = reinterpret_cast<const u32x2*>(A.xB + (tm * 128 + wr * 64 + fq * 4) * 1024 + nt * 128 + wc * 64) + fr;
    const u32x2 dw = reinterpret_cast<const u32x2*>(A.Dsk + nt * 128 + wc * 64)[fr];
    u32x2 xw[16];
#pragma unroll
    for (int i = 0; i < 16; ++i) xw[i] = px[((i >> 2) * 16 + (i & 3)) * 256];
#pragma unroll
    for (int i = 0; i < 32; ++i) {
      const uint32_t a = xw[i >> 1][i & 1], d = dw[i & 1];
      const uint32_t p = pk_bf(u2f(a << 16) * u2f(d << 16), u2f(a & 0xFFFF0000u) * u2f(d & 0xFFFF0000u));
      acc[i >> 3][(i & 1) * 2][(i >> 1) & 3] = u2f(p << 16);
      acc[i >> 3][(i & 1) * 2 + 1][(i >> 1) & 3] = u2f(p & 0xFFFF0000u);
    }
#pragma unroll
    for (int m = 0; m < 4; ++m) asm volatile("" : "+v"(acc[m][0]), "+v"(acc[m][1]), "+v"(acc[m][2]), "+v"(acc[m][3]));
    lds_wait<SW_G0E, 2>(U, 4);
#pragma nounroll
    for (int k = 0; k < 3; ++k) {
      if (k == 2) lds_wait<SW_GRP, 2>(U, 4);
      stage_c(A, wave, lane, k);
    }
    wait_vm<0>();
  } else {
#pragma unroll
    for (int i = 0; i < 16; ++i) acc[i >> 2][i & 3] = Z4;
  }
  {
    hw = lds_peek<SW_HALF>(U);
    const uint64_t base = T.late ? (uint64_t)A.out : (uint64_t)ps_of(ws, hw, tm, nt, wave);
    __builtin_amdgcn_s_setprio(1);
    run1(acc, lds_base(smem) + G0_STAGE + wr * 8192 + ((fr >> 1) & 1) * 1024 + (8 * fq + (fr >> 2) + (fr & 2) * 2) * 16 + (fr & 1) * 8,
         lds_base(smem) + G0_STAGE + 16384 + (wc * 64 + fr) * 128 + (fq ^ (fr & 7)) * 16, T.late ? 32 - NX : NX,
         T.late ? lds_base(smem) + ((29 - NX) & 3) * G0_STAGE + wave * 8192 : 0,
         u32x4{(uint32_t)base, (uint32_t)(base >> 32) & 0xFFFFu, 16u << 20, 0x00020000u},
         T.late ? ((tm * 128 + wr * 64) * 2048 + 1024 + nt * 128 + wc * 64) * 4 : 0);
  }
  if (!T.late) {
    lane = lane_id();
    lds_signal<SW_PL>(U);
    publish(counter(ws, 64 + bid_x()), lane);
    warm_dev(smem, ps_of(ws, hw, tm, nt, wave), lane * 128);
  }
}

}

extern "C" __global__ __launch_bounds__(1024, 4) void k_all(const uint32_t* x2, const unsigned short* w_r, const unsigned short* w_i,
                                                           const unsigned short* xB, const unsigned short* Lre,
                                                           const unsigned short* Lim, const unsigned short* Bre,
                                                           const unsigned short* Bim, const unsigned short* Cre,
                                                           const unsigned short* Cim, const unsigned short* ldt,
                                                           const unsigned short* Dsk, float* out, char* ws) {
  __shared__ __attribute__((aligned(256))) char smem_[LDS_BYTES];
  char* const smem = smem_ + 256;
  const Args A{x2, w_r, w_i, xB, Lre, Lim, Bre, Bim, Cre, Cim, ldt, Dsk, out, ws};
  const int tid = tid_x(), lane = tid & 63;
  int wave = __builtin_amdgcn_readfirstlane(tid >> 6);
  const uint32_t ldsb = lds_base(smem);
  {
    const int o = lane * 128 + (lane >> 5) * ((bid_x() << 9) & 0x7000);
    uint32_t t0, t1, t2, t3;
    asm volatile(
        "s_cmp_ge_u32 %[w], 8\n\ts_cbranch_scc1 8f\n\t"
        "s_load_dwordx2 s[64:65], %[ka], 0\n\ts_load_dwordx2 s[66:67], %[ka], 24\n\ts_load_dwordx4 s[68:71], %[ka], 48\n\t"
        "s_cmp_lg_u32 %[w], 0\n\ts_cbranch_scc1 1f\n\ts_getpc_b64 s[72:73]\n\ts_add_u32 s72, s72, k_all@rel32@lo+4\n\t"
        "s_addc_u32 s73, s73, k_all@rel32@hi+12\n\ts_mov_b32 m0, %[scr]\n\ts_nop 0\n\tglobal_load_lds_dword %[o], s[72:73]\n"
        "1:\n\tv_lshrrev_b32 %0, 3, %[l]\n\tv_and_b32 %1, 7, %[l]\n\ts_lshl_b32 s72, %[w], 12\n\t"
        "s_cmp_lt_u32 %[b], 128\n\ts_cbranch_scc1 2f\n\t"
        "s_add_u32 m0, s72, %[s3]\n\ts_lshl_b32 s72, %[w], 19\n\ts_and_b32 s73, %[b], 7\n\ts_lshl_b32 s73, s73, 11\n\ts_add_u32 s72, s72, s73\n\t"
        "s_lshl_b32 s73, %[b], 4\n\ts_and_b32 s73, s73, 0x780\n\ts_add_u32 s72, s72, s73\n\t"
        "v_lshrrev_b32 %2, 1, %0\n\tv_xor_b32 %1, %1, %2\n\tv_lshlrev_b32 %1, 4, %1\n\tv_lshl_add_u32 %0, %0, 14, %1\n\t"
        "v_add_u32 %0, s72, %0\n\tv_xor_b32 %1, 64, %0\n\tv_add_u32 %1, 130048, %1\n\tv_add_u32 %2, 260096, %0\n\t"
        "v_add_u32 %3, 260096, %1\n\ts_waitcnt lgkmcnt(0)\n\t"
        "global_load_lds_dwordx4 %0, s[64:65] nt\n\tglobal_load_lds_dwordx4 %1, s[64:65] offset:1024 nt\n\t"
        "global_load_lds_dwordx4 %2, s[64:65] offset:2048 nt\n\tglobal_load_lds_dwordx4 %3, s[64:65] offset:3072 nt\n\ts_branch 9f\n"
        "2:\n\ts_add_u32 m0, s72, 256\n\ts_mul_i32 s73, %[b], 1280\n\ts_and_b32 s73, s73, 0x600\n\t"
        "s_cmp_ge_u32 %[w], 4\n\ts_cbranch_scc1 3f\n\t"
        "s_lshl_b32 s72, %[b], 17\n\ts_and_b32 s72, s72, 0x3c0000\n\ts_add_u32 s73, s73, s72\n\ts_lshl_b32 s72, %[w], 16\n\t"
        "s_add_u32 s73, s73, s72\n\tv_and_b32 %2, 3, %0\n\tv_xor_b32 %1, %1, %2\n\tv_lshlrev_b32 %1, 4, %1\n\t"
        "v_lshl_add_u32 %0, %0, 11, %1\n\tv_add_u32 %0, s73, %0\n\tv_add_u32 %1, 15360, %0\n\tv_xor_b32 %2, 64, %0\n\t"
        "v_add_u32 %3, 46080, %2\n\tv_add_u32 %2, 30720, %2\n\ts_waitcnt lgkmcnt(0)\n\t"
        "global_load_lds_dwordx4 %0, s[66:67]\n\tglobal_load_lds_dwordx4 %1, s[66:67] offset:1024\n\t"
        "global_load_lds_dwordx4 %2, s[66:67] offset:2048\n\tglobal_load_lds_dwordx4 %3, s[66:67] offset:3072\n\ts_branch 9f\n"
        "3:\n\ts_and_b32 s72, %[b], 1\n\ts_lshl_b32 s72, s72, 19\n\ts_add_u32 s73, s73, s72\n\ts_lshl_b32 s72, %[b], 12\n\t"
        "s_and_b32 s72, s72, 0x60000\n\ts_add_u32 s73, s73, s72\n\ts_and_b32 s72, %[w], 3\n\ts_lshl_b32 s72, s72, 15\n\t"
        "s_add_u32 s73, s73, s72\n\tv_xor_b32 %1, %1, %0\n\tv_lshlrev_b32 %1, 4, %1\n\tv_lshl_add_u32 %0, %0, 11, %1\n\t"
        "v_add_u32 %0, s73, %0\n\tv_add_u32 %1, 15360, %0\n\ts_waitcnt lgkmcnt(0)\n\t"
        "global_load_lds_dwordx4 %0, s[68:69]\n\tglobal_load_lds_dwordx4 %1, s[68:69] offset:1024\n\ts_add_u32 m0, m0, 2048\n\ts_nop 0\n\t"
        "global_load_lds_dwordx4 %0, s[70:71]\n\tglobal_load_lds_dwordx4 %1, s[70:71] offset:1024\n\ts_branch 9f\n"
        "8:\n\ts_cmp_lg_u32 %[w], 15\n\ts_cbranch_scc1 9f\n\ts_setprio 3\n\t"
        "s_load_dwordx4 s[64:67], %[ka], 32\n\ts_load_dwordx2 s[68:69], %[ka], 80\n\t"
        "s_and_b32 s72, %[b], 1\n\ts_lshl_b32 s72, s72, 9\n\ts_lshl_b32 s73, %[b], 2\n\ts_and_b32 s73, s73, 0x180\n\ts_add_u32 s72, s72, s73\n\t"
        "s_and_b32 s73, %[b], 128\n\ts_lshl_b32 s73, s73, 3\n\ts_add_u32 s72, s72, s73\n\tv_lshl_add_u32 %0, %[l], 2, s72\n\t"
        "s_mov_b32 m0, %[prm]\n\ts_mov_b32 exec_hi, 0\n\ts_waitcnt lgkmcnt(0)\n\tglobal_load_lds_dword %0, s[64:65]\n\t"
        "s_add_u32 m0, m0, 128\n\ts_nop 0\n\tglobal_load_lds_dword %0, s[66:67]\n\ts_add_u32 m0, m0, 128\n\ts_nop 0\n\t"
        "global_load_lds_dword %0, s[68:69]\n\ts_mov_b32 exec_hi, -1\n\ts_setprio 0\n9:"
        : "=&v"(t0), "=&v"(t1), "=&v"(t2), "=&v"(t3)
        : [l] "v"(lane), [o] "v"(o < 36488 - 16 ? o : 36488 - 16), [w] "s"(wave), [b] "s"(bid_x()),
          [ka] "s"(__builtin_amdgcn_kernarg_segment_ptr()), [scr] "n"(256 + L_SCR2), [s3] "n"(256 + 3 * G0_STAGE), [prm] "n"(256 + L_PRM + 512)
        : "s64", "s65", "s66", "s67", "s68", "s69", "s70", "s71", "s72", "s73", "scc", "memory");
  }
  if (wave < 8) {
  } else if (wave < 12) {
    const int wc = (bid_x() >> 3) & 15;
    if (bid_x() >= 128 && (wc & 7) != 3 && wave != 10)
      warm_r(A, smem, lane, wave == 11 ? (wc & 8) | 3 : wc, wave == 11 ? (wc & 7) - ((wc & 7) > 3) : wave - 8);
  } else if (wave == 13) {
    warm1(smem, Dsk, (lane & 15) * 128);
  }
  asm volatile("" : "+s"(wave));
  bool g1 = false;
  if (__builtin_expect(wave >= 12, 0)) {
    if (wave == 12) *(volatile lds_u32*)((lds_u32*)(smem + L_SYNC) + lane) = 0;
    mfma_role(A, smem, wave - 12, lane);
    g1 = bid_x() < 128;
  } else if (__builtin_expect(wave >= 8, 0)) {
    loader_role(A, smem, wave - 8, lane);
  } else {
    g1 = fft_role(A, smem, wave, lane);
  }
  if (g1) gemm1(A, smem, wave & 3, (int)__builtin_amdgcn_mbcnt_hi(~0u, __builtin_amdgcn_mbcnt_lo(~0u, 0u)));
}
#if defined(__HIP_DEVICE_COMPILE__) && __HIP_DEVICE_COMPILE__
#pragma clang attribute pop
#endif

extern "C" void kernel_launch(void* const* d_in, const int* in_sizes, int n_in, void* d_out, int out_size, void* d_ws,
                              size_t ws_size, hipStream_t stream) {
  typedef const unsigned short* P;
  k_all<<<256, 1024, 0, stream>>>((const uint32_t*)d_in[0], (P)d_in[1], (P)d_in[2], (P)d_in[3], (P)d_in[4], (P)d_in[5], (P)d_in[6],
                                  (P)d_in[7], (P)d_in[8], (P)d_in[9], (P)d_in[10], (P)d_in[11], (float*)d_out, (char*)d_ws);
}
```

```cpp
#include <hip/hip_runtime.h>
#include <stdint.h>

namespace {

#if defined(__HIP_DEVICE_COMPILE__) && __HIP_DEVICE_COMPILE__
#pragma clang attribute push(__attribute__((target("no-packed-fp32-ops"))), apply_to = function)
#endif
__device__ __forceinline__ int tid_x() { return __builtin_amdgcn_workitem_id_x(); }
__device__ __forceinline__ int bid_x() { return __builtin_amdgcn_workgroup_id_x(); }

typedef float cf __attribute__((ext_vector_type(2)));
typedef short bf16x8 __attribute__((ext_vector_type(8)));
typedef short s16x4 __attribute__((ext_vector_type(4)));
typedef _Float16 f16x8 __attribute__((ext_vector_type(8)));
typedef _Float16 f16x2 __attribute__((ext_vector_type(2)));
typedef __bf16 b16x2 __attribute__((ext_vector_type(2)));
typedef float f32x4 __attribute__((ext_vector_type(4)));
typedef uint32_t u32x4 __attribute__((ext_vector_type(4)));
typedef uint32_t u32x2 __attribute__((ext_vector_type(2)));
typedef unsigned short u16;
typedef unsigned long long u64;
typedef __attribute__((address_space(3))) uint32_t lds_u32;

__device__ __forceinline__ float u2f(uint32_t u) { return __builtin_bit_cast(float, u); }
__device__ __forceinline__ uint32_t f2u(float f) { return __builtin_bit_cast(uint32_t, f); }
__device__ __forceinline__ float bf2f(u16 b) { return u2f((uint32_t)b << 16); }
__device__ __forceinline__ float rbf(float f) { return bf2f(__builtin_bit_cast(u16, (__bf16)f)); }
__device__ __forceinline__ uint32_t pk_bf(float a, float b) {
  const b16x2 h = b16x2{(__bf16)a, (__bf16)b};
  return __builtin_bit_cast(uint32_t, h);
}
__device__ __forceinline__ uint32_t pk_h(float a, float b) {
  const f16x2 h = f16x2{(_Float16)a, (_Float16)b};
  return __builtin_bit_cast(uint32_t, h);
}
__device__ __forceinline__ cf fin(cf v) {
  float x = v.x, y = v.y;
  asm("" : "+v"(x), "+v"(y));
  return cf{x, y};
}
__device__ __forceinline__ cf cmul(cf a, cf b) {
  return cf{__builtin_fmaf(a.x, b.x, -(a.y * b.y)), __builtin_fmaf(a.x, b.y, a.y * b.x)};
}
__device__ __forceinline__ cf cmad(cf a, cf b, cf c) {
  return cf{__builtin_fmaf(a.x, b.x, __builtin_fmaf(-a.y, b.y, c.x)), __builtin_fmaf(a.x, b.y, __builtin_fmaf(a.y, b.x, c.y))};
}
__device__ __forceinline__ cf cisr(int k, float inv_n) {
  const float r = (float)k * inv_n;
  return cf{__builtin_amdgcn_cosf(r), -__builtin_amdgcn_sinf(r)};
}
template <int N> __device__ __forceinline__ cf cis(int k) { return cisr(k, 1.0f / N); }
template <int N> __device__ __forceinline__ void wait_vm() { asm volatile("s_waitcnt vmcnt(%0)" ::"n"(N) : "memory"); }
__device__ __forceinline__ void wait_lgkm0() { asm volatile("s_waitcnt lgkmcnt(0)" ::: "memory"); }
__device__ __forceinline__ void glds2_dev(const void* sb, uint32_t v0, uint32_t lds_dst) {
  uint32_t keep;
  asm volatile(
      "s_mov_b32 %0, m0\n\ts_mov_b32 m0, %3\n\ts_nop 0\n\tglobal_load_lds_dwordx4 %1, %2 sc1\n\t"
      "global_load_lds_dwordx4 %1, %2 offset:1024 sc1\n\ts_mov_b32 m0, %0"
      : "=&s"(keep)
      : "v"(v0), "s"(sb), "s"(lds_dst)
      : "memory");
}
__device__ __forceinline__ void gdev2(const void* sb, uint32_t v0, uint32_t v1, uint32_t lds_dst) {
  uint32_t keep;
  asm volatile(
      "s_mov_b32 %0, m0\n\ts_mov_b32 m0, %4\n\ts_nop 0\n\tglobal_load_lds_dwordx4 %1, %3 sc1\n\t"
      "global_load_lds_dwordx4 %2, %3 offset:1024 sc1\n\ts_mov_b32 m0, %0"
      : "=&s"(keep)
      : "v"(v0), "v"(v1), "s"(sb), "s"(lds_dst)
      : "memory");
}
__device__ __forceinline__ void glds4(const void* sb0, const void* sb1, uint32_t v0, uint32_t v1, uint32_t v2, uint32_t v3,
                                      uint32_t lds_dst) {
  uint32_t keep;
  asm volatile(
      "s_mov_b32 %0, m0\n\ts_mov_b32 m0, %7\n\ts_nop 0\n\tglobal_load_lds_dwordx4 %1, %5\n\t"
      "global_load_lds_dwordx4 %2, %5 offset:1024\n\tglobal_load_lds_dwordx4 %3, %6 offset:2048\n\t"
      "global_load_lds_dwordx4 %4, %6 offset:3072\n\ts_mov_b32 m0, %0"
      : "=&s"(keep)
      : "v"(v0), "v"(v1), "v"(v2), "v"(v3), "s"(sb0), "s"(sb1), "s"(lds_dst)
      : "memory");
}
__device__ __forceinline__ constexpr uint32_t lds_base(char*) { return 256; }
__device__ __forceinline__ void bar() {
  __builtin_amdgcn_sched_barrier(0);
  asm volatile("" ::: "memory");
  __builtin_amdgcn_s_barrier();
  asm volatile("" ::: "memory");
  __builtin_amdgcn_sched_barrier(0);
}
__device__ __forceinline__ void bar_lds() {
  asm volatile("s_waitcnt lgkmcnt(0)" ::: "memory");
  bar();
}
template <int W> __device__ __forceinline__ void lds_signal(uint32_t U) {
  asm volatile("s_mov_b64 exec, 1\n\tds_add_u32 %0, %0 offset:%1\n\ts_mov_b64 exec, -1" ::"v"(U), "n"(W * 4 - 4) : "memory");
}
template <int W> __device__ __forceinline__ uint32_t lds_peek(uint32_t U) {
  return __builtin_amdgcn_readfirstlane(*(volatile lds_u32*)(size_t)(U + (W * 4 - 4)));
}
template <int W, int SLEEP> __device__ __forceinline__ void lds_wait(uint32_t U, uint32_t n) {
  uint32_t v, c;
  if (SLEEP)
    asm volatile(
        "s_branch 2f\n1:\n\ts_sleep %5\n2:\n\tds_read_b32 %0, %2 offset:%4\n\ts_waitcnt lgkmcnt(0)\n\tv_readfirstlane_b32 %1, %0\n\t"
        "s_cmp_lt_u32 %1, %3\n\ts_cbranch_scc1 1b"
        : "=&v"(v), "=&s"(c)
        : "v"(U), "s"(n * U), "n"(W * 4 - 4), "n"(SLEEP)
        : "memory", "scc");
  else
    asm volatile(
        "1:\n\tds_read_b32 %0, %2 offset:%4\n\ts_waitcnt lgkmcnt(0)\n\tv_readfirstlane_b32 %1, %0\n\t"
        "s_cmp_lt_u32 %1, %3\n\ts_cbranch_scc1 1b"
        : "=&v"(v), "=&s"(c)
        : "v"(U), "s"(n * U), "n"(W * 4 - 4)
        : "memory", "scc");
}
__device__ __forceinline__ int lane_id() {
  int l;
  asm volatile("v_mbcnt_lo_u32_b32 %0, -1, 0\n\tv_mbcnt_hi_u32_b32 %0, -1, %0" : "=v"(l));
  return l;
}
__device__ __forceinline__ uint32_t wave_max_u32(uint32_t v) {
  asm volatile(
      "s_nop 1\n\tv_max_u32_dpp %0, %0, %0 quad_perm:[1,0,3,2] row_mask:0xf bank_mask:0xf\n\t"
      "s_nop 1\n\tv_max_u32_dpp %0, %0, %0 quad_perm:[2,3,0,1] row_mask:0xf bank_mask:0xf\n\t"
      "s_nop 1\n\tv_max_u32_dpp %0, %0, %0 row_half_mirror row_mask:0xf bank_mask:0xf\n\t"
      "s_nop 1\n\tv_max_u32_dpp %0, %0, %0 row_mirror row_mask:0xf bank_mask:0xf\n\t"
      "s_nop 1\n\tv_max_u32_dpp %0, %0, %0 row_bcast:15 row_mask:0xa bank_mask:0xf\n\t"
      "s_nop 1\n\tv_max_u32_dpp %0, %0, %0 row_bcast:31 row_mask:0xc bank_mask:0xf\n\ts_nop 1"
      : "+v"(v));
  return (uint32_t)__builtin_amdgcn_readlane((int)v, 63);
}
__device__ __forceinline__ float sel_f(uint64_t m, float a, float b) {
  float r;
  asm("v_cndmask_b32_e64 %0, %1, %2, %3" : "=v"(r) : "v"(b), "v"(a), "s"(m));
  return r;
}
__device__ __forceinline__ cf sel_c(uint64_t m, cf a, cf b) { return cf{sel_f(m, a.x, b.x), sel_f(m, a.y, b.y)}; }
__device__ __forceinline__ u64 ld_dev(const u64* p) { return __hip_atomic_load(p, __ATOMIC_RELAXED, __HIP_MEMORY_SCOPE_AGENT); }
__device__ __forceinline__ void st_dev(u64* p, u64 v) { __hip_atomic_store(p, v, __ATOMIC_RELAXED, __HIP_MEMORY_SCOPE_AGENT); }
__device__ __forceinline__ void st_dev(uint32_t* p, uint32_t v) {
  __hip_atomic_store(p, v, __ATOMIC_RELAXED, __HIP_MEMORY_SCOPE_AGENT);
}
__device__ __forceinline__ f32x4 mfma_h(u32x4 a, u32x4 b, f32x4 c) {
  return __builtin_amdgcn_mfma_f32_16x16x32_f16(__builtin_bit_cast(f16x8, a), __builtin_bit_cast(f16x8, b), c, 0, 0, 0);
}
__device__ __forceinline__ f32x4 mfma_b(u32x4 a, u32x4 b, f32x4 c) {
  return __builtin_amdgcn_mfma_f32_16x16x32_bf16(__builtin_bit_cast(bf16x8, a), __builtin_bit_cast(bf16x8, b), c, 0, 0, 0);
}
struct PQ {
  u32x4 p, q;
};
constexpr f32x4 Z4 = f32x4{0.f, 0.f, 0.f, 0.f};
constexpr unsigned SPIN_MAX = 1u << 20;

constexpr size_t WS_EPOCH = 0;
constexpr size_t WS_F1 = 4096;
constexpr size_t WS_P2 = 8192;
constexpr size_t WS_HF = 1u << 19;
constexpr size_t WS_AGG = 1u << 20;
constexpr size_t WS_ZB = 2u << 20;
constexpr size_t WS_PS = 8u << 20;
constexpr size_t WS_PG = 16u << 20;
__device__ __forceinline__ uint32_t half_of(uint32_t w, int tm, int i) { return (((w >> tm) ^ ((w >> (16 + tm)) & i)) & 1) * 8192; }
__device__ __forceinline__ char* counter(char* ws, int i) { return ws + WS_HF + i * 128; }
__device__ __forceinline__ char* ps_of(char* ws, uint32_t w, int tm, int nt, int wave) {
  const int i = nt * 4 + wave;
  return i < 30 ? ws + WS_PG + tm * (2 << 20) + (3 << 19) + i * 16384 + half_of(w, tm, wave) : ws + WS_PS + (tm * 2 + i) * 8192;
}
__device__ __forceinline__ int agg_off(int lane) { return lane * 16 + (lane >= 24 ? 128 : 0); }

constexpr int NX = 12;
constexpr int G0_STAGE = 32768;
constexpr int L_Z = 0;
constexpr int L_X = 131072;
constexpr int L_TB = L_X + 16384;
constexpr int L_WS = L_TB + 4096;
constexpr int L_PART = L_WS + 4096;
constexpr int L_GRP = L_PART + 3584;
constexpr int L_RS = L_GRP + 2048;
constexpr int L_SYNC = -256;
constexpr int L_PRM = L_RS + 1024;
constexpr int L_SCR2 = L_PRM + 1024;
constexpr int LDS_BYTES = L_SCR2 + 256 + 256;
enum { SW_GRP = 2, SW_PUB, SW_WMAX, SW_XMAX, SW_HD, SW_G1, SW_FS, SW_G0E, SW_HS, SW_LD, SW_RS, SW_F1, SW_PX, SW_FR, SW_PL, SW_P2, SW_N0, SW_N1, SW_HALF };

struct Args {
  const uint32_t* x2;
  const u16 *w_r, *w_i, *xB, *Lre, *Lim, *Bre, *Bim, *Cre, *Cim, *ldt, *Dsk;
  float* out;
  char* ws;
};

struct Tile {
  int tm, tn, nt, late;
};
__device__ __forceinline__ Tile tile_map() {
  const int bid = bid_x(), late = bid >> 7, cq = (bid & 1) * 4 + ((bid >> 5) & 3);
  return Tile{(bid >> 1) & 15, late * 8 + cq, cq, late};
}
__device__ __forceinline__ u32x4 lds4(const void* p) { return *reinterpret_cast<const u32x4*>(p); }
__device__ __forceinline__ u32x4 ldsa(uint32_t a) { return *(__attribute__((address_space(3))) u32x4*)(size_t)a; }
__device__ __forceinline__ void lds_cf2(const void* p, cf& a, cf& b) {
  const f32x4 v = *reinterpret_cast<const f32x4*>(p);
  a = cf{v[0], v[1]};
  b = cf{v[2], v[3]};
}
template <int W> __device__ __forceinline__ void group_sync(uint32_t U, uint32_t n) {
  wait_lgkm0();
  lds_signal<W>(U);
  lds_wait<W, 0>(U, n);
}
__device__ __forceinline__ int k_rot() { return (bid_x() & 6) * 2 + (bid_x() & 1) * 8; }
__device__ __forceinline__ void stage0(const Args& A, char* smem, int wave, int lane, int kt) {
  const Tile T = tile_map();
  const int prow = lane >> 3;
  const uint32_t va = (T.tm * 128 + wave * 32 + prow) * 2048 + ((lane & 7) ^ (prow & 3)) * 16;
  const uint32_t vb = (T.tn * 64 + wave * 16 + prow) * 2048 + ((lane & 7) ^ prow) * 16;
  const uint32_t dst = lds_base(smem) + wave * 4096 + (kt & 3) * G0_STAGE;
  kt = (kt + k_rot()) & 15;
  glds4(A.xB + kt * 64, A.xB + kt * 64, va, va + 15360, (va ^ 64) + 2 * 15360, (va ^ 64) + 3 * 15360, dst);
  glds4(A.Bre + kt * 64, A.Bim + kt * 64 - 1024, vb, vb + 15360, vb, vb + 15360, dst + 16384);
}
__device__ __forceinline__ int g1_kt(int late, int s) {
  const int e = late ? s - 2 : s, o = 2 * tile_map().nt, r = late ? s - 10 : s + 14 - NX;
  return (unsigned)e < 8u ? ((e + 6) & 7) + (late ? 0 : 8) : (e < 0 ? o + s : (r < o ? r : r + 2)) + 16;
}
__device__ __forceinline__ uint32_t g1_vc(int wave, int lane) {
  return (tile_map().nt * 128 + (wave >> 1) * 64 + (wave & 1) * 2 + (lane >> 3) * 4) * 2048 + ((lane & 7) ^ (lane >> 3)) * 16;
}
__device__ __forceinline__ void stage_c(const Args& A, int wave, int lane, int s) {
  const int kt = g1_kt(tile_map().late, s);
  const u16* src = ((kt & 1) ? A.Cim : A.Cre) + (kt >> 1) * 64;
  const uint32_t vc = g1_vc(wave, lane);
  glds4(src, src, vc, vc + 65536 - 1024, vc, vc + 65536 - 1024, 256 + ((s + 1) & 3) * G0_STAGE + 16384 + wave * 4096);
}
__device__ __forceinline__ void warm1(char* smem, const void* base, uint32_t off) {
  uint32_t keep;
  asm volatile("s_mov_b32 %0, m0\n\ts_mov_b32 m0, %3\n\ts_nop 0\n\tglobal_load_lds_dword %1, %2\n\ts_mov_b32 m0, %0"
               : "=&s"(keep)
               : "v"(off), "s"(base), "s"(lds_base(smem) + L_SCR2)
               : "memory");
}
__device__ __forceinline__ void warm_dev(char* smem, const void* base, uint32_t off) {
  uint32_t keep;
  asm volatile("s_mov_b32 %0, m0\n\ts_mov_b32 m0, %3\n\ts_nop 0\n\tglobal_load_lds_dword %1, %2 sc1\n\ts_mov_b32 m0, %0"
               : "=&s"(keep)
               : "v"(off), "s"(base), "s"(lds_base(smem) + L_SCR2)
               : "memory");
}
typedef short s16x2 __attribute__((ext_vector_type(2)));
__device__ __forceinline__ uint32_t pmax(uint32_t m, uint32_t u) {
  return __builtin_bit_cast(uint32_t, __builtin_elementwise_max(__builtin_bit_cast(s16x2, m), __builtin_bit_cast(s16x2, u & 0x7FFF7FFFu)));
}
template <int W> __device__ __forceinline__ void lds_max(uint32_t U, uint32_t m) {
  const uint32_t h = m << 16;
  m = h > m ? h : m;
  asm volatile(
      "s_nop 1\n\tv_max_u32_dpp %0, %0, %0 quad_perm:[1,0,3,2] row_mask:0xf bank_mask:0xf\n\t"
      "s_nop 1\n\tv_max_u32_dpp %0, %0, %0 quad_perm:[2,3,0,1] row_mask:0xf bank_mask:0xf\n\t"
      "s_nop 1\n\tv_max_u32_dpp %0, %0, %0 row_half_mirror row_mask:0xf bank_mask:0xf\n\t"
      "s_mov_b64 exec, %3\n\tds_max_u32 %1, %0 offset:%2\n\ts_mov_b64 exec, -1"
      : "+v"(m)
      : "v"(U), "n"(W * 4 - 4), "s"(0x0101010101010101ull)
      : "memory");
}
__device__ __forceinline__ void warm_r(const Args& A, char* smem, int lane, int c, int i) {
  const int bid = bid_x(), tm = ((bid >> 1) & 3) + (c & 3) * 4, nt = (bid & 1) * 4 + (c >> 2);
  uint64_t px = (uint64_t)A.xB, pbr = (uint64_t)A.Bre, pbi = (uint64_t)A.Bim;
  asm volatile("" : "+s"(px), "+s"(pbr), "+s"(pbi));
  px += (tm * 128 + (nt & 3) * 32 + (lane & 31)) * 2048;
  const uint64_t pb = ((lane & 16) ? pbi : pbr) + (nt * 64 + (c & 3) * 16 + (lane & 15)) * 2048;
  const int hi = lane >> 5;
  const bool k2 = i == 4 || i == 6;
  const int sl = i < 3 ? i + 1 : (k2 ? i - 4 : i + 1) + hi;
  const uint64_t pfp = ((k2 || (i < 3 && hi)) ? pb + (k2 ? 1 << 20 : 0) : px) + ((sl + k_rot()) & 15) * 128;
  uint32_t keep;
  asm volatile("s_mov_b32 %0, m0\n\ts_mov_b32 m0, %2\n\ts_nop 0\n\tglobal_load_lds_dword %1, off\n\ts_mov_b32 m0, %0"
               : "=&s"(keep)
               : "v"(pfp), "s"(lds_base(smem) + L_SCR2)
               : "memory");
}
__device__ __forceinline__ uint32_t pk_mul_h(uint32_t a, uint32_t b) {
  return __builtin_bit_cast(uint32_t, __builtin_bit_cast(f16x2, a) * __builtin_bit_cast(f16x2, b));
}
__device__ __forceinline__ u32x2 lds_tr(uint32_t a) {
  return __builtin_bit_cast(u32x2, __builtin_amdgcn_ds_read_tr16_b64_v4i16((__attribute__((address_space(3))) s16x4*)(size_t)a));
}
__device__ __forceinline__ void sta4(uint32_t a, u32x4 v) { *(__attribute__((address_space(3))) u32x4*)(size_t)a = v; }
template <int P> __device__ __forceinline__ void prio(bool on) {
  __builtin_amdgcn_sched_barrier(0);
  if (on) __builtin_amdgcn_s_setprio(P);
  __builtin_amdgcn_sched_barrier(0);
}
__device__ __forceinline__ void row_head(u32x4 (&x)[2][2], const PQ& m16, const PQ& m1, const cf (&tw)[2][4], bool on) {
  f32x4 dr[2][2], di[2][2];
#pragma unroll
  for (int k = 0; k < 2; ++k) {
    const f32x4 ur = mfma_h(x[k][0], m16.p, Z4), ui = mfma_h(x[k][0], m16.q, Z4);
    const u32x4 xn = x[k][1] ^ 0x80008000u;
    dr[k][0] = mfma_h(x[k][1], m1.p, ur), di[k][0] = mfma_h(x[k][1], m1.q, ui);
    dr[k][1] = mfma_h(xn, m1.p, ur), di[k][1] = mfma_h(xn, m1.q, ui);
  }
  prio<0>(on);
#pragma unroll
  for (int i = 0; i < 16; ++i) {
    const int k = i >> 3, t = (i >> 2) & 1, r = i & 3;
    const cf v = fin(cmul(cf{dr[k][t][r], di[k][t][r]}, tw[t][r]));
    dr[k][t][r] = v.x, di[k][t][r] = v.y;
  }
  prio<1>(on);
#pragma unroll
  for (int i = 0; i < 16; ++i) x[i >> 3][(i >> 2) & 1][i & 3] = pk_h(dr[i >> 3][(i >> 2) & 1][i & 3], di[i >> 3][(i >> 2) & 1][i & 3]);
}
template <int W, bool B = false> __device__ __forceinline__ void poll_count(char* p, uint32_t n, uint32_t U) {
  uint32_t c0, c1, c2, t, lim = SPIN_MAX;
#define ASK(c) "global_atomic_add " c ", %5, %5, %6 sc0\n\t"
#define CHK(c) "s_waitcnt vmcnt(%10)\n\tv_readfirstlane_b32 %3, " c "\n\ts_cmp_ge_u32 %3, %7\n\ts_cbranch_scc1 2f\n\t" ASK(c)
  asm volatile(
      "s_mov_b64 exec, 1\n\t" ASK("%0") "s_sleep 4\n\t" ASK("%1") "s_sleep 4\n\t" ASK("%2") ".if %11\n\ts_barrier\n.endif\n1:\n\t" CHK("%0") CHK("%1") CHK("%2")
      "s_sub_u32 %4, %4, 1\n\ts_cbranch_scc0 1b\n2:\n\tds_add_u32 %8, %8 offset:%9\n\ts_mov_b64 exec, -1\n\ts_waitcnt vmcnt(0)"
      : "=&v"(c0), "=&v"(c1), "=&v"(c2), "=&s"(t), "+s"(lim)
      : "v"(0), "s"(p), "s"(n), "v"(U), "n"(W * 4 - 4), "n"(2), "n"(B)
      : "memory", "scc");
#undef ASK
#undef CHK
}
__device__ __forceinline__ bool fft_role(const Args& A, char* smem, int f, int lane) {
  char* const ws = A.ws;
  const Tile T = tile_map();
  const int bid = bid_x(), li = bid & 127;
  int l15 = lane & 15, q = lane >> 4;
  lds_u32* const sw = (lds_u32*)(smem + L_SYNC);
  const uint32_t U = (uint32_t)(size_t)(sw + 1), ldsb = lds_base(smem);
  const __amdgpu_buffer_rsrc_t rzb = __builtin_amdgcn_make_buffer_rsrc((void*)(ws + WS_ZB), 0, 48 << 20, 0x00020000);
  auto fsync = [&](int n) { group_sync<SW_FS>(U, 8 * n); };
  auto bars = [&](int n) {
#pragma nounroll
    for (int i = 0; i < n; ++i) bar();
  };
  u64 epoch = 0;
  if (f == 0) epoch = ld_dev((const u64*)(ws + WS_EPOCH));
  if (f == 7) {
    const __amdgpu_buffer_rsrc_t ra = __builtin_amdgcn_make_buffer_rsrc((void*)(ws + WS_AGG), 0, 1 << 20, 0x00020000);
    __builtin_amdgcn_raw_buffer_store_b128(u32x4{0u, 0u, 0u, 0u}, ra, (T.tm * 32 + T.tn) * 2048 + agg_off(lane), 0, 16);
    __builtin_amdgcn_raw_buffer_store_b128(u32x4{0u, 0u, 0u, 0u}, ra, (T.tm * 32 + 16 + T.tn) * 2048 + agg_off(lane), 0, 16);
    if (!T.late && lane < (T.tn ? 1 : 4)) st_dev((uint32_t*)counter(ws, lane ? 4 * T.tm + lane - 1 : 64 + li), 0u);
    if (lane == 0) st_dev((uint32_t*)(ws + WS_P2) + bid, 0u);
  }
  __builtin_amdgcn_sched_barrier(0);
  auto ks_of = [&](int slot) {
    const int w = slot & 7, k = bid ? bid + 256 * w : (w < 4 ? 256 * w : 256 * w - 896);
    return slot < 8 ? k : (k ? 2048 - k : 1024);
  };
  {
    const int e = f & 3, n = 4 * q + e;
    uint32_t* tb = reinterpret_cast<uint32_t*>(smem + L_TB) + lane * 4;
    uint32_t* tx = reinterpret_cast<uint32_t*>(smem + L_X) + lane * 4;
    cf c = cis<256>(n * l15 * (f < 4 ? 16 : 1));
    if (f < 4) {
      c = c * 0.25f;
      tb[e] = pk_h(c.x, -c.y);
      tb[256 + e] = pk_h(c.y, c.x);
      if (T.late) {
        const uint32_t ph = pk_bf(c.x, -c.y), qh = pk_bf(c.y, c.x);
        const float lr = c.x - u2f(ph << 16), lo = c.y - u2f(qh << 16);
        tx[e] = ph;
        tx[256 + e] = qh;
        tx[512 + e] = pk_bf(lr, -lo);
        tx[768 + e] = pk_bf(lo, lr);
      } else {
        const cf d = cis<32>((2 * n + 1) * l15) * 0.25f;
        tx[e] = pk_h(d.x, -d.y);
        tx[256 + e] = pk_h(d.y, d.x);
      }
    } else {
      *reinterpret_cast<cf*>(tb + 512 + (e >> 1) * 256 + (e & 1) * 2) = c;
    }
    if (!T.late) {
      *reinterpret_cast<cf*>(tx + 512 + (f >> 1) * 256 + (f & 1) * 2) = cis<512>(n * (16 * (f >> 2) + l15)) * 0.5f;
      const cf w = cis<1024>(l15 + 16 * (f >> 2) + 32 * n);
      *reinterpret_cast<cf*>(tx + 1536 + (f >> 1) * 256 + (f & 1) * 2) = cf{w.y, -w.x};
      const int slot = f < 4 ? l15 : n, u = f < 4 ? q >> 1 : l15 & 1, j = f < 4 ? n & 7 : l15 >> 1;
      const cf g = cis<2048>(j * ks_of(slot)) * (((bid ? slot >> 3 : (slot >> 2) & 1) == u) ? 0.5f : 0.f);
      const uint32_t cj = slot < 8 ? 0u : 0x80000000u;
      tx[2560 + (f >> 2) * 512 + e] = pk_h(g.x, -g.y) ^ (f < 4 ? 0u : cj);
      tx[2816 + (f >> 2) * 512 + e] = pk_h(g.y, g.x) ^ (f < 4 ? cj | cj >> 16 : cj);
    }
  }
  if (!T.late) {
    if (f == 0) wait_vm<1>(); else if (f == 7) wait_vm<4>(); else wait_vm<0>();
  }
  bar_lds();
  PQ m16, m1, m8;
  cf tw[2][4];
  u32x4 wv[4];
  uint32_t pay = 0;
  m16.p = lds4(smem + L_TB + lane * 16), m16.q = lds4(smem + L_TB + 1024 + lane * 16);
  if (T.late) {
    const char* tab = smem + L_X + lane * 16;
    const uint32_t xl = ldsb + 3 * G0_STAGE + f * 4096 + lane * 16;
    const uint32_t xc = ldsb + 3 * G0_STAGE + (l15 + 64 * q) * 128 + (f ^ (l15 >> 1)) * 16;
    PQ mh, ml;
    mh.p = lds4(tab), mh.q = lds4(tab + 1024), ml.p = lds4(tab + 2048), ml.q = lds4(tab + 3072);
    lds_cf2(smem + L_TB + 2048 + lane * 16, tw[0][0], tw[0][1]), lds_cf2(smem + L_TB + 3072 + lane * 16, tw[0][2], tw[0][3]);
    if (f == 0) wait_vm<1>(); else if (f == 7) wait_vm<3>(); else wait_vm<0>();
    {
      uint32_t mx[2] = {0, 0};
#pragma unroll
      for (int i = 0; i < 4; ++i) wv[i] = ldsa(xl + i * 1024);
#pragma unroll
      for (int i = 0; i < 16; ++i) mx[i & 1] = pmax(mx[i & 1], wv[i >> 2][i & 3]);
      lds_max<SW_XMAX>(U, pmax(mx[0], mx[1]));
    }
    fsync(1);
#pragma unroll
    for (int e = 0; e < 4; ++e) wv[e] = ldsa(xc + e * 2048);
    int ecu = (int)(lds_peek<SW_XMAX>(U) >> 23);
    ecu = ecu < 26 ? 26 : ecu;
    pay = (uint32_t)ecu;
    float sc = u2f((uint32_t)(257 - ecu) << 23);
    asm volatile("" : "+v"(sc));
#pragma unroll
    for (int r = 0; r < 4; ++r) tw[0][r] = tw[0][r] * sc;
    f32x4 dr[4], di[4];
#pragma unroll
    for (int c = 0; c < 4; ++c) {
      const u32x4 x = u32x4{wv[0][c], wv[1][c], wv[2][c], wv[3][c]};
      dr[c] = mfma_b(x, mh.p, mfma_b(x, ml.p, Z4));
      di[c] = mfma_b(x, mh.q, mfma_b(x, ml.q, Z4));
    }
#pragma unroll
    for (int i = 0; i < 16; ++i) {
      const cf v = fin(cmul(cf{dr[i >> 2][i & 3], di[i >> 2][i & 3]}, tw[0][i & 3]));
      dr[i >> 2][i & 3] = v.x, di[i >> 2][i & 3] = v.y;
    }
#pragma unroll
    for (int c = 0; c < 4; ++c) {
      const u32x4 a = u32x4{pk_h(dr[c][0], di[c][0]), pk_h(dr[c][1], di[c][1]), pk_h(dr[c][2], di[c][2]), pk_h(dr[c][3], di[c][3])};
      dr[c] = mfma_h(m16.p, a, Z4);
      di[c] = mfma_h(m16.q, a, Z4);
    }
#pragma unroll
    for (int r = 0; r < 4; ++r)
      sta4(xc + r * 2048, u32x4{pk_h(dr[0][r], di[0][r]), pk_h(dr[1][r], di[1][r]), pk_h(dr[2][r], di[2][r]), pk_h(dr[3][r], di[3][r])});
    fsync(2);
#pragma unroll
    for (int i = 0; i < 4; ++i) wv[i] = ldsa(xl + i * 1024);
    const int zo = (32 * f + (lane >> 3)) * 16384 + (li & 7) * 2048 + (li >> 3) * 128 + ((lane & 7) ^ q) * 16;
    asm volatile("" : "+v"(wv[0]), "+v"(wv[1]), "+v"(wv[2]), "+v"(wv[3]));
#pragma unroll
    for (int i = 0; i < 4; ++i) __builtin_amdgcn_raw_buffer_store_b128(wv[i], rzb, (zo ^ ((i & 1) * 64)) + i * 131072, 0, 16);
    bar();
  } else {
    const char* tab = smem + L_X + lane * 16;
    m1.p = lds4(tab), m1.q = lds4(tab + 1024), m8.p = lds4(tab + 10240), m8.q = lds4(tab + 11264);
#pragma unroll
    for (int i = 0; i < 4; ++i) lds_cf2(tab + 2048 + i * 1024, tw[i >> 1][(i & 1) * 2], tw[i >> 1][(i & 1) * 2 + 1]);
  }
  wait_vm<0>();
  asm volatile("" : "+v"(epoch));
  const u64 tag = ((epoch + 1) & 0x7FFFFFFFFFFFull) | 0x800000000000ull;
  if (T.late && f == 0) pay |= lds_peek<SW_N1>(U) > lds_peek<SW_N0>(U) ? 256u : 0u;
  u64 f1 = (tag << 16) | (u64)pay;
  asm volatile("" : "+v"(f1));
  bar();
  if (f == 0 && lane == 0) st_dev((u64*)(ws + WS_F1) + bid, f1);
  int nb = f == 0 && !T.late ? 10 : 11;
  if (T.late && f >= 6) {
    uint64_t pbr = (uint64_t)A.Bre, pbi = (uint64_t)A.Bim;
    asm volatile("" : "+s"(pbr), "+s"(pbi));
    uint64_t pf = ((lane & 16) ? pbi : pbr) + (T.tn * 64 + (T.tm >> 2) * 16 + l15) * 2048 + ((2 * f + (lane >> 5) + k_rot()) & 15) * 128;
    asm volatile("" : "+v"(pf));
    bars(f - 3);
    nb -= f - 3;
    __builtin_amdgcn_s_sleep(5);
    uint32_t keep;
    asm volatile("s_mov_b32 %0, m0\n\ts_mov_b32 m0, %2\n\ts_nop 0\n\tglobal_load_lds_dword %1, off\n\ts_mov_b32 m0, %0"
                 : "=&s"(keep)
                 : "v"(pf), "s"(ldsb + L_SCR2)
                 : "memory");
  }
  bars(nb);
  int zo = (q < 2 ? bid : (bid ? 256 - bid : 128)) * 16384 + (q & 1) * 8192 + f * 256 + l15 * 16;
  asm volatile("" : "+v"(zo));
  uint32_t ex = 0, e2 = 0, e3 = 0;
  if (f == 0) {
    u64 v[4];
    auto poll1 = [&]() {
#pragma unroll
      for (int i = 0; i < 4; ++i) v[i] = ld_dev((const u64*)(ws + WS_F1) + lane + 64 * i);
    };
    __builtin_amdgcn_s_sleep(5);
    poll1();
    if (!T.late) bar();
    bar();
    __builtin_amdgcn_s_setprio(2);
#pragma nounroll
    for (unsigned spins = 0; spins < SPIN_MAX; ++spins) {
      bool ok = true;
#pragma unroll
      for (int i = 0; i < 4; ++i) ok &= (v[i] >> 16) == tag;
      if (__builtin_amdgcn_ballot_w64(!ok) == 0) break;
      __builtin_amdgcn_s_sleep(16);
      poll1();
    }
    lds_signal<SW_F1>(U);
    e2 = (uint32_t)v[2] & 511u, e3 = (uint32_t)v[3] & 511u;
    asm volatile("" : "+v"(e2), "+v"(e3));
  } else {
    if (T.late && f >= 4) {
      __builtin_amdgcn_s_sleep(6);
      warm1(smem, (f & 1) ? A.Cre : A.Cim, (T.nt * 32 + T.tm * 2 + ((f >> 1) & 1)) * 8192 + lane * 128);
    }
    bar();
  }
  u32x4 zi[4];
  if (!T.late) {
    if (f) lds_wait<SW_F1, 1>(U, 1);
#pragma unroll
    for (int e = 0; e < 4; ++e) zi[e] = __builtin_amdgcn_raw_buffer_load_b128(rzb, zo + e * 2048, 0, 16);
  }
  if (f == 0) {
    const uint64_t b2 = __builtin_amdgcn_ballot_w64(e2 >= 256u), b3 = __builtin_amdgcn_ballot_w64(e3 >= 256u);
    auto two_of = [&](uint32_t w) { return __builtin_popcount((w >> 2 * l15) & 3u); };
    const int n = two_of(b2) + two_of(b2 >> 32) + two_of(b3) + two_of(b3 >> 32);
    const uint32_t hw = ((uint32_t)__builtin_amdgcn_ballot_w64(n >= 6) & 0xFFFFu) | (uint32_t)__builtin_amdgcn_ballot_w64(n > 2 && n < 6) << 16;
    *(volatile lds_u32*)(sw + SW_HALF) = hw;
    e2 &= 255u, e3 &= 255u;
    ex = wave_max_u32(e2 > e3 ? e2 : e3);
    const float s2 = __builtin_amdgcn_ldexpf(1.0f, (int)e2 - (int)ex), s3 = __builtin_amdgcn_ldexpf(1.0f, (int)e3 - (int)ex);
    uint32_t* rs = reinterpret_cast<uint32_t*>(smem + L_RS) + lane;
    rs[0] = pk_h(s2, s2);
    rs[64] = pk_h(s3, s3);
    wait_lgkm0();
    lds_signal<SW_RS>(U);
    __builtin_amdgcn_s_setprio(0);
  }
  if (f == 0 && lane == 0) {
    if (T.late) st_dev((uint32_t*)(ws + WS_P2) + bid, 256u);
    if (bid == 0) st_dev((u64*)(ws + WS_EPOCH), epoch + 1);
  }
  if ((bid | f) == 0) __builtin_amdgcn_s_setprio(3);
  if (!T.late) {
    const __amdgpu_buffer_rsrc_t rr = __builtin_amdgcn_make_buffer_rsrc((void*)A.w_r, 0, 2048 * 1026, 0x00020000);
    const __amdgpu_buffer_rsrc_t ri = __builtin_amdgcn_make_buffer_rsrc((void*)A.w_i, 0, 2048 * 1026, 0x00020000);
    uint32_t sr, si;
    auto wload = [&](int ka) {
      const int oa = ka * 1026, ob = ((2048 - ka) & 2047) * 1026, o1 = (ka & 1) ? oa - 2 : ob, o0 = (ka & 1) ? ob + 2 : oa;
      wv[0] = __builtin_amdgcn_raw_buffer_load_b128(rr, lane * 16, o1, 2);
      wv[1] = __builtin_amdgcn_raw_buffer_load_b128(ri, lane * 16, o1, 2);
      wv[2] = __builtin_amdgcn_raw_buffer_load_b128(rr, lane * 16, o0, 2);
      wv[3] = __builtin_amdgcn_raw_buffer_load_b128(ri, lane * 16, o0, 2);
      const int so = (int)f2u(sel_f(1, u2f(ob), sel_f(2, u2f(oa + 1024), u2f(o1 + 1024))));
      sr = __builtin_amdgcn_raw_buffer_load_b16(rr, so, 0, 2);
      si = __builtin_amdgcn_raw_buffer_load_b16(ri, so, 0, 2);
    };
    const bool two = (bid | f) == 0;
    u32x4 w2r, w2i;
    uint32_t s2r, s2i;
    if (two) {
      wload(1024);
      w2r = wv[2], w2i = wv[3], s2r = sr, s2i = si;
    }
    wload(ks_of(f));
    const uint32_t row = ldsb + L_Z + f * 2048;
    uint32_t wa = row, wb = two ? ldsb + L_X + 14336 : row + 16384;
    uint32_t ra = row + q * 512 + ((l15 >> 2) ^ f ^ ((q & 1) << 2)) * 16 + (l15 & 3) * 4;
    uint32_t w1 = ((bid & 1) ? wa : wb) + lane * 16, w0 = ((bid & 1) ? wb : wa) + lane * 16;
    uint32_t za = ldsb + L_Z + l15 * 2048 + f * 256 + ((4 * q) ^ (l15 & 7) ^ ((f & 2) << 1)) * 16;
    asm volatile("" : "+v"(ra), "+v"(w1), "+v"(w0), "+v"(za));
    __builtin_amdgcn_sched_barrier(0);
    lds_wait<SW_RS, 1>(U, 1);
    {
      const u32x4 rs = lds4(smem + L_RS + f * 64 + (l15 >> 3) * 32 + (q & 1) * 16);
      u32x4 o[4];
      asm volatile("" : "+v"(zi[0]), "+v"(zi[1]), "+v"(zi[2]), "+v"(zi[3]));
#pragma unroll
      for (int c = 0; c < 4; ++c) {
        u32x4 a;
#pragma unroll
        for (int e = 0; e < 4; ++e) a[e] = pk_mul_h(zi[e][c], rs[e]);
        const f32x4 re = mfma_h(a, m8.p, Z4), im = mfma_h(a, m8.q, Z4);
#pragma unroll
        for (int r = 0; r < 4; ++r) o[r][c] = pk_h(re[r], im[r]);
      }
#pragma unroll
      for (int r = 0; r < 4; ++r) sta4(za ^ (r * 16), o[r]);
    }
    __builtin_amdgcn_sched_barrier(0);
    uint32_t wsp;
    {
      auto shift = [](u32x4& w, uint32_t last) {
        const uint32_t nx = (uint32_t)__builtin_amdgcn_update_dpp((int)last, (int)(uint32_t)w[0], 0x130, 0xF, 0xF, false);
        w = u32x4{__builtin_amdgcn_alignbit(w[1], w[0], 16), __builtin_amdgcn_alignbit(w[2], w[1], 16),
                  __builtin_amdgcn_alignbit(w[3], w[2], 16), __builtin_amdgcn_alignbit(nx, w[3], 16)};
      };
      uint32_t mx[4] = {0, 0, 0, 0};
#pragma unroll
      for (int i = 0; i < 8; ++i) mx[i & 1] = pmax(mx[i & 1], wv[2 + (i >> 2)][i & 3]);
      shift(wv[0], sr), shift(wv[1], si);
      wsp = sr | si << 16;
      if (two) {
        sta4(ldsb + L_WS + lane * 16, w2r), sta4(ldsb + L_WS + 1024 + lane * 16, w2i);
#pragma unroll
        for (int i = 0; i < 4; ++i) mx[0] = pmax(mx[0], w2r[i]), mx[1] = pmax(mx[1], w2i[i]);
        shift(w2r, s2r), shift(w2i, s2i);
        sta4(ldsb + L_WS + 2048 + lane * 16, w2r), sta4(ldsb + L_WS + 3072 + lane * 16, w2i);
        wsp = (uint32_t)__builtin_amdgcn_update_dpp((int)wsp, (int)(s2r | s2i << 16), 0x114, 0xF, 0x2, false);
      }
#pragma unroll
      for (int i = 0; i < 8; ++i) mx[2 + (i & 1)] = pmax(mx[2 + (i & 1)], wv[i >> 2][i & 3]);
      lds_max<SW_WMAX>(U, pmax(pmax(pmax(mx[0], wsp), mx[1]), pmax(mx[2], mx[3])));
    }
    fsync(1);
    prio<1>(!two);
    u32x4 x[2][2];
    auto rows = [&]() {
#pragma unroll
      for (int i = 0; i < 16; ++i) x[i >> 3][(i >> 2) & 1][i & 3] = *(lds_u32*)(size_t)((ra ^ ((i & 4) * 16)) + (i & 3) * 128 + (i >> 3) * 16384);
    };
    rows();
    pay = lds_peek<SW_WMAX>(U) >> 23;
    pay = pay < 26u ? 26u : pay;
    float fs = __builtin_amdgcn_ldexpf(0.25f, 125 - (int)pay);
    if (two) x[1][0] = x[0][0] ^ 0x80000000u, x[1][1] = x[0][1] ^ 0x80000000u;
    asm volatile("" : "+v"(x[0][0]), "+v"(x[0][1]), "+v"(x[1][0]), "+v"(x[1][1]), "+v"(fs)::"memory");
    sta4(w1, wv[0]);
    sta4(w1 + 1024, wv[1]);
    sta4(w0, wv[2]);
    sta4(w0 + 1024, wv[3]);
#pragma nounroll
    for (int jb = 0;; jb = 4) {
      u32x2 fw[4][2];
#pragma unroll
      for (int h = 0; h < 2; ++h) {
        fw[0][h] = lds_tr(wa + q * 256 + l15 * 8 + h * 128);
        fw[1][h] = lds_tr(wa + q * 256 + l15 * 8 + h * 128 + 1024);
        fw[2][h] = lds_tr(wb + (3 - q) * 256 + l15 * 8 + h * 128);
        fw[3][h] = lds_tr(wb + (3 - q) * 256 + l15 * 8 + h * 128 + 1024);
      }
      row_head(x, m16, m1, tw, !two);
#pragma unroll
      for (int i = 0; i < 8; ++i)
        fw[2 + (i >> 2)][(i >> 1) & 1][i & 1] =
            (uint32_t)__builtin_amdgcn_update_dpp(0, (int)(uint32_t)fw[2 + (i >> 2)][(i >> 1) & 1][i & 1], 0x140, 0xF, 0xF, true);
      const uint32_t s0 = __builtin_amdgcn_readlane(wsp, jb), s1 = __builtin_amdgcn_readlane(wsp, jb + 1);
#pragma unroll
      for (int t = 0; t < 2; ++t) {
        cf u[4];
        lds_cf2(smem + L_X + 6144 + t * 2048 + lane * 16, u[0], u[1]), lds_cf2(smem + L_X + 7168 + t * 2048 + lane * 16, u[2], u[3]);
        f32x4 ar = mfma_h(m16.p, x[0][t], Z4), ai = mfma_h(m16.q, x[0][t], Z4), br = mfma_h(m16.p, x[1][t], Z4), bi = mfma_h(m16.q, x[1][t], Z4);
        auto half = [](uint32_t w, int hi) { return u2f(hi ? w & 0xFFFF0000u : w << 16); };
        cf lo[4];
#pragma unroll
        for (int r = 0; r < 4; ++r) {
          const int rr = t ? 3 - r : r;
          lo[r] = fin(cf{half(fw[2 * t][rr >> 1][rr & 1], 0), half(fw[2 * t + 1][rr >> 1][rr & 1], 0)});
        }
        prio<0>(!two);
#pragma unroll
        for (int r = 0; r < 4; ++r) {
          const int rr = t ? r : 3 - r;
          const cf hi = cf{half(fw[2 - 2 * t][rr >> 1][rr & 1], 1), half(fw[3 - 2 * t][rr >> 1][rr & 1], 1)};
          cf fa = t ? hi : lo[r], fb = t ? lo[r] : hi;
          fb.y = -fb.y;
          if (t == 0 && r == 0 && lane == 0) {
            fa = (fa + cf{half(s0, 0), -half(s0, 1)}) * 0.5f;
            fb = (fb + cf{half(s1, 0), half(s1, 1)}) * 0.5f;
          }
          const cf tb = cf{br[r] * fs, bi[r] * fs};
          const cf E = cf{__builtin_fmaf(ar[r], fs, tb.x), __builtin_fmaf(ai[r], fs, tb.y)};
          const cf D = cf{__builtin_fmaf(ar[r], fs, -tb.x), __builtin_fmaf(ai[r], fs, -tb.y)};
          const cf X1 = cmad(u[r], D, E), X2 = cf{__builtin_fmaf(2.f, E.x, -X1.x), __builtin_fmaf(2.f, E.y, -X1.y)};
          const cf Yb = cmul(X2, fb), S = cmad(X1, fa, Yb);
          const cf Df = cf{__builtin_fmaf(-2.f, Yb.x, S.x), __builtin_fmaf(-2.f, Yb.y, S.y)};
          const cf O1 = fin(cmad(cf{u[r].x, -u[r].y}, Df, S));
          const cf O2 = fin(cf{__builtin_fmaf(2.f, S.x, -O1.x), __builtin_fmaf(-2.f, S.y, O1.y)});
          ar[r] = O1.x, ai[r] = O1.y, br[r] = O2.x, bi[r] = O2.y;
        }
        prio<1>(!two);
#pragma unroll
        for (int r = 0; r < 4; ++r) x[0][t][r] = pk_h(ar[r], -ai[r]), x[1][t][r] = pk_h(br[r], bi[r]);
      }
      if (!two) break;
      if (jb) {
        x[0][0] = ldsa(row + lane * 16), x[0][1] = ldsa(row + 1024 + lane * 16);
        break;
      }
      sta4(row + lane * 16, x[0][0]), sta4(row + 1024 + lane * 16, x[0][1]);
      rows();
      x[0][0] = x[1][0] ^ 0x80000000u, x[0][1] = x[1][1] ^ 0x80000000u;
      wa = ldsb + L_WS, wb = ldsb + L_WS + 2048;
    }
    row_head(x, m16, m1, tw, !two);
    const uint32_t oa = row + l15 * 128 + (q ^ (l15 & 7)) * 16;
#pragma unroll
    for (int i = 0; i < 4; ++i) {
      const f32x4 re = mfma_h(x[i >> 1][i & 1], m16.p, Z4), im = mfma_h(x[i >> 1][i & 1], m16.q, Z4);
      sta4((oa ^ ((i & 1) * 64)) + (i >> 1) * 16384, u32x4{pk_h(re[0], im[0]), pk_h(re[1], im[1]), pk_h(re[2], im[2]), pk_h(re[3], im[3])});
    }
    m8.p = lds4(smem + L_X + 12288 + lane * 16), m8.q = lds4(smem + L_X + 13312 + lane * 16);
    fsync(2);
    u32x4 yi[4];
#pragma unroll
    for (int e = 0; e < 4; ++e) yi[e] = lds4(smem + L_Z + (4 * q + e) * 2048 + f * 256 + (l15 ^ ((2 * f + (l15 >> 3)) & 7)) * 16);
    asm volatile("" : "+v"(yi[0]), "+v"(yi[1]), "+v"(yi[2]), "+v"(yi[3]));
    u32x4 o[4];
#pragma unroll
    for (int c = 0; c < 4; ++c) {
      const u32x4 y = u32x4{yi[0][c], yi[1][c], yi[2][c], yi[3][c]};
      const f32x4 re = mfma_h(m8.p, y, Z4), im = mfma_h(m8.q, y, Z4);
#pragma unroll
      for (int r = 0; r < 4; ++r) o[r][c] = pk_h(re[r], im[r]);
    }
    const int ct = (8 * f + 4 * (l15 >> 3) + q) & 15, k1 = bid ? 256 - bid : 128;
    const int zc = (int)(WS_PG - WS_ZB) + ct * (2 << 20) + (1 << 20) + (f >> 1) * 65536 + (l15 & 7) * 16;
    const uint32_t hw = lds_peek<SW_HALF>(U);
    int z0 = zc + half_of(hw, ct, bid >> 6), z1 = zc + half_of(hw, ct, k1 >> 6);
    asm volatile("" : "+v"(o[0]), "+v"(o[1]), "+v"(o[2]), "+v"(o[3]), "+v"(z0), "+v"(z1));
    lds_wait<SW_HD, 1>(U, 4);
#pragma unroll
    for (int i = 0; i < 4; ++i) {
      const int k = (i & 1) ? k1 : bid;
      __builtin_amdgcn_raw_buffer_store_b128(o[i], rzb, ((i & 1) ? z1 : z0) + (i >> 1) * 262144 + (k >> 6) * 16384 + (k & 63) * 128, 0, 16);
    }
    __builtin_amdgcn_s_setprio(0);
  } else {
    if (f >= 4) return true;
    if (f == 1) {
      lds_wait<SW_PUB, 4>(U, 4);
      lds_wait<SW_F1, 4>(U, 1);
      __builtin_amdgcn_s_sleep(10);
      poll_count<SW_G1>(counter(ws, 4 * T.tm), 16, U);
    }
  }
  const int N = T.late ? 32 - NX : NX;
  if (T.late && f != 1) return false;
  bars(2);
  wait_vm<0>();
  __builtin_amdgcn_s_waitcnt(0x0F70);
  bar();
  lane = lane_id();
  uint32_t vo = T.tm * (2 << 20) + (1 << 20) + T.nt * 65536 + (f >> 1) * 16384 + half_of(lds_peek<SW_HALF>(U), T.tm, f >> 1) + (f & 1) * 4096 +
                (lane >> 3) * 128 + ((lane & 7) ^ (lane >> 4)) * 16;
  uint32_t vp = vo ^ 64;
  asm volatile("" : "+v"(vo), "+v"(vp));
  u32x4 pw;
  auto scales = [&]() {
    uint32_t mw = pw[0] > pw[1] ? pw[0] : pw[1], m2 = pw[2] > pw[3] ? pw[2] : pw[3];
    mw = wave_max_u32(mw > m2 ? mw : m2);
#pragma unroll
    for (int i = 0; i < 4; ++i) {
      const float sc = __builtin_amdgcn_ldexpf(1.0f, (int)(uint32_t)pw[i] - (int)mw);
      pw[i] = pk_h(sc, sc);
    }
    if (lane == 32) pw[0] = ex + mw;
    if (lane < 33) sta4(ldsb + L_RS + lane * 16, pw);
    wait_lgkm0();
    lds_signal<SW_RS>(U);
  };
  if (f == 1) {
    bars(T.late ? 3 : 1);
    auto ask = [&](char* p) {
      uint32_t c;
      asm volatile("s_mov_b64 exec, 1\n\tglobal_atomic_add %0, %1, %1, %2 sc0\n\ts_mov_b64 exec, -1\n\ts_barrier\n\ts_waitcnt vmcnt(0)"
                   : "=&v"(c)
                   : "v"(0), "s"(p)
                   : "memory");
      return (uint32_t)__builtin_amdgcn_readfirstlane(c);
    };
    if (ask(counter(ws, 4 * T.tm + 2)) < 32u) poll_count<SW_FR>(counter(ws, 4 * T.tm + 2), 32, U);
    if (T.late) {
      bars(N - 11);
      if (ask(counter(ws, 64 + li)) >= 4u) {
        bar();
        lds_signal<SW_PX>(U);
      } else {
        poll_count<SW_PX, true>(counter(ws, 64 + li), 4, U);
      }
      return false;
    }
    bars(N - 7);
  } else if (f == 0) {
    if (lane == 0) st_dev((uint32_t*)(ws + WS_P2) + bid, 256u | pay);
    bars(N - 7);
    const __amdgpu_buffer_rsrc_t rp = __builtin_amdgcn_make_buffer_rsrc((void*)(ws + WS_P2), 0, 1024, 0x00020000);
    pw = __builtin_amdgcn_raw_buffer_load_b128(rp, lane * 16, 0, 16);
    bars(2);
#pragma nounroll
    for (unsigned spins = 0; spins < SPIN_MAX; ++spins) {
      if (__builtin_amdgcn_ballot_w64((pw[0] & pw[1] & pw[2] & pw[3] & 256u) == 0) == 0) break;
      __builtin_amdgcn_s_sleep(8);
      pw = __builtin_amdgcn_raw_buffer_load_b128(rp, lane * 16, 0, 16);
      asm volatile("" ::: "memory");
    }
    lds_signal<SW_P2>(U);
    asm volatile("" : "+v"(pw));
  } else {
    bars(N - 5);
  }
  {
    constexpr uint32_t img = 256 + ((NX - 3) & 3) * G0_STAGE;
    if (f) lds_wait<SW_P2, 1>(U, 1);
    gdev2(ws + WS_PG, vo, vp, img + f * 4096);
    gdev2(ws + WS_PG + 2048, vo, vp, img + f * 4096 + 2048);
    if (f == 0) scales();
    lane = lane_id();
    l15 = lane & 15;
    q = lane >> 4;
    uint32_t xc = img + (l15 + 64 * q) * 128 + (f ^ (l15 >> 1)) * 16;
    uint32_t xr = img + (f & 3) * 8192 + (lane >> 3) * 128 + (((f & 4) + ((lane & 7) >> 1)) ^ q) * 16 + (lane & 1) * 8;
    int oo = (64 * (f & 3) + (lane >> 3)) * 65536 + (li & 7) * 8192 + (li >> 3) * 256 + (f >> 2) * 128 + (lane & 7) * 16;
    lds_cf2(smem + L_TB + 2048 + lane * 16, tw[0][0], tw[0][1]), lds_cf2(smem + L_TB + 3072 + lane * 16, tw[0][2], tw[0][3]);
    asm volatile("" : "+v"(xc), "+v"(xr), "+v"(oo), "+v"(tw[0][0]), "+v"(tw[0][1]), "+v"(tw[0][2]), "+v"(tw[0][3]));
    lds_wait<SW_RS, 1>(U, 2);
    u32x4 wv[4], rs;
#pragma unroll
    for (int e = 0; e < 4; ++e) {
      const int k = l15 + 64 * q + 16 * e;
      rs[e] = *(lds_u32*)(size_t)(ldsb + L_RS + ((q < 2 ? k : -k) & 127) * 4);
    }
    const int sh = __builtin_amdgcn_readfirstlane((int)*(lds_u32*)(size_t)(ldsb + L_RS + 512)) - 511;
    float sc = __builtin_amdgcn_ldexpf(1.0f, sh);
    asm volatile("" : "+v"(rs), "+v"(sc));
    wait_vm<0>();
    fsync(3);
#pragma unroll
    for (int e = 0; e < 4; ++e) wv[e] = ldsa(xc + e * 2048);
    f32x4 dr[4], di[4];
#pragma unroll
    for (int c = 0; c < 4; ++c) {
      const u32x4 x = u32x4{pk_mul_h(wv[0][c], rs[0]), pk_mul_h(wv[1][c], rs[1]), pk_mul_h(wv[2][c], rs[2]), pk_mul_h(wv[3][c], rs[3])};
      dr[c] = mfma_h(x, m16.p, Z4);
      di[c] = mfma_h(x, m16.q, Z4);
    }
#pragma unroll
    for (int c = 0; c < 4; ++c) {
      u32x4 a;
#pragma unroll
      for (int r = 0; r < 4; ++r) {
        const cf v = fin(cmul(cf{dr[c][r], di[c][r]}, tw[0][r]));
        a[r] = pk_h(v.x, v.y);
      }
      dr[c] = mfma_h(m16.p, a, Z4);
      di[c] = mfma_h(m16.q, a, Z4);
    }
    auto put = [&](auto scale) {
#pragma unroll
      for (int r = 0; r < 4; ++r) {
        u32x4 y;
#pragma unroll
        for (int c = 0; c < 4; ++c) y[c] = pk_bf(scale(dr[c][r]), scale(-di[c][r]));
        sta4(xc + r * 2048, y);
      }
    };
    if (__builtin_expect(sh >= -126 && sh <= 127, 1)) put([&](float x) { return x * sc; });
    else put([&](float x) { return __builtin_amdgcn_ldexpf(x, sh); });
    if (f < 4) group_sync<SW_N0>(U, 4);
    else group_sync<SW_N1>(U, 4);
    typedef __attribute__((address_space(3))) u32x2 lds_u32x2;
    u32x2 z[8];
#pragma unroll
    for (int i = 0; i < 8; ++i) z[i] = *(lds_u32x2*)(size_t)((xr ^ ((i & 1) * 64)) + i * 1024);
    const __amdgpu_buffer_rsrc_t ro = __builtin_amdgcn_make_buffer_rsrc((void*)A.out, 0, 16 << 20, 0x00020000);
    lds_wait<SW_PL, 2>(U, 4);
#pragma unroll
    for (int i = 0; i < 8; ++i) {
      const uint32_t p0 = z[i][0], p1 = z[i][1];
      __builtin_amdgcn_raw_buffer_store_b128(u32x4{p0 << 16, p0 & 0xFFFF0000u, p1 << 16, p1 & 0xFFFF0000u}, ro, oo + i * 524288, 0, 2);
    }
    if (f == 0 && lane_id() < 2) st_dev((u64*)(ws + WS_F1) + bid + lane_id() * 128, 0ull);
  }
  return false;
}

__device__ __forceinline__ void publish(char* p, int lane) {
  wait_vm<0>();
  if (lane == 0) __hip_atomic_fetch_add((uint32_t*)p, 1u, __ATOMIC_RELAXED, __HIP_MEMORY_SCOPE_AGENT);
}
__device__ __forceinline__ void loader_role(const Args& A, char* smem, int wave, int lane) {
  char* const ws = A.ws;
  const Tile T = tile_map();
  const int tm = T.tm, tn = T.tn;
  lds_u32* const sw = (lds_u32*)(smem + L_SYNC);
  const uint32_t U = (uint32_t)(size_t)(sw + 1);
  const uint32_t ldsb = lds_base(smem), lds0 = ldsb + wave * 4096;
  const int prow = lane >> 3;
  {
    __builtin_amdgcn_s_setprio(0);
    const uint32_t va = (tm * 128 + wave * 32 + prow) * 2048 + ((lane & 7) ^ (prow & 3)) * 16;
    const uint32_t vb = (tn * 64 + wave * 16 + prow) * 2048 + ((lane & 7) ^ prow) * 16;
    if (T.late) {
      bar();
      const int wc = (bid_x() >> 3) & 15;
      if ((wc & 7) != 3) {
#pragma nounroll
        for (int i = 2 + wave; i < 7; i += 4) warm_r(A, smem, lane, wc, i);
      }
      stage0(A, smem, wave, lane, 0);
    }
    stage0(A, smem, wave, lane, 1);
    stage0(A, smem, wave, lane, 2);
#pragma nounroll
    for (int k = 3; k < 16; ++k) {
      const uint32_t dst = lds0 + (k & 3) * G0_STAGE;
      const int ko = ((k + k_rot()) & 15) * 64;
      asm volatile(
          "s_mov_b32 m0, %9\n\ts_waitcnt vmcnt(8)\n\ts_barrier\n\t"
          "global_load_lds_dwordx4 %0, %6\n\tglobal_load_lds_dwordx4 %1, %6 offset:1024\n\t"
          "global_load_lds_dwordx4 %2, %6 offset:2048\n\tglobal_load_lds_dwordx4 %3, %6 offset:3072\n\t"
          "s_mov_b32 m0, %10\n\ts_nop 0\n\t"
          "global_load_lds_dwordx4 %4, %7\n\tglobal_load_lds_dwordx4 %5, %7 offset:1024\n\t"
          "global_load_lds_dwordx4 %4, %8 offset:2048\n\tglobal_load_lds_dwordx4 %5, %8 offset:3072"
          ::"v"(va), "v"(va + 15360), "v"((va ^ 64) + 2 * 15360), "v"((va ^ 64) + 3 * 15360), "v"(vb), "v"(vb + 15360),
            "s"(A.xB + ko), "s"(A.Bre + ko), "s"(A.Bim + ko - 1024), "s"(dst), "s"(dst + 16384)
          : "memory");
    }
    wait_vm<8>();
    bar();
    wait_vm<0>();
    lds_signal<SW_LD>(U);
  }
  __builtin_amdgcn_s_setprio(2);
  {
    const int nseg = 2 * tm - 8 * wave;
    cf G = cf{0.f, 0.f};
    lds_wait<SW_RS, 2>(U, 1);
    if (nseg > 0) {
      const __amdgpu_buffer_rsrc_t ra = __builtin_amdgcn_make_buffer_rsrc((void*)(ws + WS_AGG), 0, 1 << 20, 0x00020000);
      int o[8];
#pragma unroll
      for (int k = 0; k < 8; ++k) {
        o[k] = ((nseg - 1 - k) * 16 + tn) * 2048 + agg_off(lane);
        asm volatile("" : "+v"(o[k]));
      }
      u32x4 u[8];
      cf l64 = reinterpret_cast<const cf*>(smem + L_PART)[192 + lane], l128 = reinterpret_cast<const cf*>(smem + L_PART)[384 + lane];
      cf lw = reinterpret_cast<const cf*>(smem + L_GRP)[wave * 64 + lane];
      asm volatile("" : "+v"(l64), "+v"(l128), "+v"(lw));
      lds_wait<SW_PUB, 1>(U, 4);
      __builtin_amdgcn_s_sleep(10);
#pragma nounroll
      for (unsigned spins = 0; spins < SPIN_MAX; ++spins) {
        uint32_t n = 0;
#pragma unroll
        for (int k = 0; k < 8; ++k) u[k] = __builtin_amdgcn_raw_buffer_load_b128(ra, o[k], 0, 16);
#pragma unroll
        for (int k = 0; k < 8; ++k) n += u[k][1] + u[k][3];
        if (__builtin_amdgcn_ballot_w64(n != 2u * (nseg < 8 ? nseg : 8)) == 0) break;
        asm volatile("" ::: "memory");
      }
#pragma unroll
      for (int k = 6; k >= 0; k -= 2)
        G = cmad(G, l128, cmad(cf{u2f(u[k + 1][0]), u2f(u[k + 1][2])}, l64, cf{u2f(u[k][0]), u2f(u[k][2])}));
      G = cmul(G, lw);
    }
    reinterpret_cast<cf*>(smem + L_GRP)[wave * 64 + lane] = G;
    wait_lgkm0();
    lds_signal<SW_GRP>(U);
  }
  if (!T.late) {
    lds_wait<SW_GRP, 2>(U, 4);
    lds_wait<SW_G0E, 2>(U, 4);
    stage_c(A, wave, lane, 0);
    lds_wait<SW_HS, 2>(U, 4);
    stage_c(A, wave, lane, 1);
    stage_c(A, wave, lane, 2);
    lds_wait<SW_HD, 2>(U, 4);
    warm1(smem, (wave & 1) ? A.Cim : A.Cre, (T.nt * 128 + (tm >> 2) * 32 + (lane & 31)) * 2048 + (10 + (wave & 2) + (lane >> 5)) * 128);
  }
  const char* ab = ws + WS_PG + tm * (2 << 20) + (wave >> 1) * (32 * 16384) + half_of(lds_peek<SW_HALF>(U), tm, wave >> 1) + (wave & 1) * 4096;
  uint32_t vh = lane * 16, vc = g1_vc(wave, lane);
  asm volatile("" : "+v"(vh), "+v"(vc));
  lds_wait<SW_G1, 1>(U, 1);
  __builtin_amdgcn_s_setprio(0);
  const int N = T.late ? 32 - NX : NX;
#pragma nounroll
  for (int k = T.late ? 2 : 0; k < 3; ++k) {
    const char* pa = ab + g1_kt(T.late, k) * 16384;
    glds2_dev(pa, vh, lds0 + (k + 1) * G0_STAGE);
    glds2_dev(pa + 2048, vh, lds0 + (k + 1) * G0_STAGE + 2048);
  }
  wait_vm<4>();
#pragma nounroll
  for (int k = 3; k < N; ++k) {
    const uint32_t dst = lds0 + ((k + 1) & 3) * G0_STAGE;
    const int kt = g1_kt(T.late, k);
    asm volatile(
        "s_mov_b32 m0, %5\n\ts_waitcnt vmcnt(8)\n\ts_barrier\n\t"
        "global_load_lds_dwordx4 %2, %4 sc1\n\tglobal_load_lds_dwordx4 %2, %4 offset:1024 sc1\n\t"
        "global_load_lds_dwordx4 %2, %4 offset:2048 sc1\n\tglobal_load_lds_dwordx4 %2, %4 offset:3072 sc1\n\t"
        "s_mov_b32 m0, %6\n\ts_nop 0\n\t"
        "global_load_lds_dwordx4 %0, %3\n\tglobal_load_lds_dwordx4 %1, %3 offset:1024\n\t"
        "global_load_lds_dwordx4 %0, %3 offset:2048\n\tglobal_load_lds_dwordx4 %1, %3 offset:3072"
        ::"v"(vc), "v"(vc + 65536 - 1024), "v"(vh), "s"(((kt & 1) ? A.Cim : A.Cre) + (kt >> 1) * 64), "s"(ab + kt * 16384), "s"(dst),
          "s"(dst + 16384)
        : "memory");
  }
  wait_vm<8>();
  bar();
  wait_vm<0>();
  lds_signal<SW_LD>(U);
  if (T.late) {
    const char* ps = ps_of(ws, lds_peek<SW_HALF>(U), tm, T.nt, wave);
    lds_wait<SW_PX, 1>(U, 1);
#pragma nounroll
    for (int i = 0; i < 4; ++i) glds2_dev(ps + i * 2048, lane * 16, ldsb + ((29 - NX) & 3) * G0_STAGE + wave * 8192 + i * 2048);
    wait_vm<0>();
    lds_signal<SW_PL>(U);
  }
}

#define W(n) "s_waitcnt lgkmcnt(" #n ")\n\t"
#define M(c, a, b) "v_mfma_f32_16x16x32_bf16 v[" #c ":" #c "+3], v[" #a ":" #a "+3], v[" #b ":" #b "+3], v[" #c ":" #c "+3]\n\t"
#define RB(d, p, o) "ds_read_b128 v[" #d ":" #d "+3], v" #p " offset:" #o "\n\t"
#define RT(d, p, o) "ds_read_b64_tr_b16 v[" #d ":" #d "+1], v" #p " offset:" #o "\n\t"
#define AD(p) "v_add_u32 v" #p ", %[adv], v" #p "\n\t"
#define SA "s_add_i32 %[k], %[k], 1\n\ts_and_b32 %[adv], %[k], 3\n\t"
#define SB "s_cmp_eq_u32 %[adv], 0\n\ts_cselect_b32 %[adv], %[m3], 0x8000\n\t"
#define F(d, p, l) "v_fmac_f32 v[" #d "], v[" #p "], v" #l "\n\t"
#define XA(d, p) F(d, p + 4, 122) F(d + 4, p + 4, 104) F(d + 8, p + 12, 123) F(d + 12, p + 12, 106)
#define XB(d, p) F(d, p, 104) F(d + 4, p, 105) F(d + 8, p + 8, 106) F(d + 12, p + 8, 107)
#define LA0 "v_mbcnt_lo_u32_b32 v108, -1, 0\n\tv_mbcnt_hi_u32_b32 v108, -1, v108\n\t"
#define LA1 "v_and_b32 v108, 15, v108\n\tv_lshl_add_u32 v108, v108, 3, %[pp]\n\t"
#define LAM "ds_read_b64 v[104:105], v108 offset:%[lo]\n\tds_read_b64 v[106:107], v108 offset:%[lo]+128\n\ts_setprio 3\n\t"
#define L16 "ds_read2_b64 v[64:67], v108 offset0:64 offset1:80\n\tds_read2_b64 v[68:71], v108 offset0:128 offset1:144\n\t"
#define NEG "v_xor_b32 v122, 0x80000000, v105\n\tv_xor_b32 v123, 0x80000000, v107\n\t"
#define SIG "v_mov_b32 v109, 4\n\ts_mov_b64 exec, 1\n\tds_add_u32 v109, v109 offset:%[e]\n\ts_mov_b64 exec, -1\n\t"
#define STEP0(C0, CN, H4, H9, H19, H30) \
  W(7) M(0, 64, C0) RB(72, 120, 512) W(7) M(0, 68, C0 + 4) RB(76, 121, 512) \
  W(7) M(4, 64, 80) W(6) M(4, 68, 84) \
  W(5) M(8, 64, 88) SA H4 W(4) M(8, 68, 92) SB \
  W(3) M(12, 64, 96) RB(64, 120, 1024) W(3) M(12, 68, 100) RB(68, 121, 1024) \
  W(3) M(16, 72, C0) W(2) M(16, 76, C0 + 4) AD(122) H9 \
  M(20, 72, 80) RB(CN, 122, 0) M(20, 76, 84) AD(123) \
  M(24, 72, 88) RB(CN + 4, 123, 0) M(24, 76, 92) \
  M(28, 72, 96) RB(72, 120, 1536) M(28, 76, 100) RB(76, 121, 1536) \
  W(5) M(32, 64, C0) W(4) M(32, 68, C0 + 4) \
  M(36, 64, 80) M(36, 68, 84) H19 \
  M(40, 64, 88) AD(120) M(40, 68, 92) AD(121) \
  M(44, 64, 96) RB(64, 120, 0) M(44, 68, 100) RB(68, 121, 0) \
  W(3) M(52, 72, 80) RB(80, 122, 2048) W(3) M(52, 76, 84) RB(84, 123, 2048) \
  M(56, 72, 88) RB(88, 122, 4096) M(56, 76, 92) RB(92, 123, 4096) \
  M(60, 72, 96) RB(96, 122, 6144) M(60, 76, 100) RB(100, 123, 6144) \
  M(48, 72, C0) H30 M(48, 76, C0 + 4)
#define HEAD0(C0) \
  RB(C0, 122, 0) RB(C0 + 4, 123, 0) RB(64, 120, 0) RB(68, 121, 0) RB(80, 122, 2048) RB(84, 123, 2048) RB(88, 122, 4096) RB(92, 123, 4096) RB(96, 122, 6144) RB(100, 123, 6144)
#define STEP1(C0, CN, H4, H9, H19, H30) \
  W(8) M(0, 64, C0) RT(72, 120, 2048) W(6) M(0, 68, C0 + 4) RT(74, 121, 2048) \
  M(4, 64, 80) RT(76, 120, 2560) W(7) M(4, 68, 84) RT(78, 121, 2560) \
  W(7) M(8, 64, 88) SA H4 W(6) M(8, 68, 92) SB \
  W(5) M(12, 64, 96) RT(64, 120, 4096) W(5) M(12, 68, 100) RT(66, 121, 4096) \
  W(4) M(16, 72, C0) RT(68, 120, 4608) W(3) M(16, 76, C0 + 4) RT(70, 121, 4608) \
  M(20, 72, 80) AD(122) H9 M(20, 76, 84) RB(CN, 122, 0) \
  M(24, 72, 88) AD(123) M(24, 76, 92) RB(CN + 4, 123, 0) \
  M(28, 72, 96) RT(72, 120, 6144) M(28, 76, 100) RT(74, 121, 6144) \
  W(6) M(32, 64, C0) RT(76, 120, 6656) W(5) M(32, 68, C0 + 4) RT(78, 121, 6656) \
  M(36, 64, 80) M(36, 68, 84) H19 \
  M(40, 64, 88) AD(120) M(40, 68, 92) AD(121) \
  M(44, 64, 96) RT(64, 120, 0) M(44, 68, 100) RT(66, 121, 0) \
  W(4) M(52, 72, 80) RT(68, 120, 512) RB(80, 122, 2048) W(4) M(52, 76, 84) RT(70, 121, 512) RB(84, 123, 2048) \
  M(56, 72, 88) RB(88, 122, 4096) M(56, 76, 92) RB(92, 123, 4096) \
  M(60, 72, 96) RB(96, 122, 6144) M(60, 76, 100) RB(100, 123, 6144) \
  M(48, 72, C0) H30 M(48, 76, C0 + 4)
#define HEAD1(C0) \
  RB(C0, 122, 0) RB(C0 + 4, 123, 0) RT(64, 120, 0) RT(66, 121, 0) RT(68, 120, 512) RB(80, 122, 2048) RT(70, 121, 512) RB(84, 123, 2048) RB(88, 122, 4096) RB(92, 123, 4096) RB(96, 122, 6144) RB(100, 123, 6144)
#define ROW1(C0) \
  W(8) M(0, 64, C0) RT(72, 120, 2048) W(6) M(0, 68, C0 + 4) RT(74, 121, 2048) \
  M(4, 64, 80) RT(76, 120, 2560) W(7) M(4, 68, 84) RT(78, 121, 2560) \
  W(7) M(8, 64, 88) W(6) M(8, 68, 92) \
  W(5) M(12, 64, 96) W(4) M(12, 68, 100) \
  W(0)
#define TAIL0 \
  W(7) M(0, 64, 112) RB(72, 120, 512) LA0 W(7) M(0, 68, 116) RB(76, 121, 512) LA1 \
  W(7) M(4, 64, 80) LAM W(8) M(4, 68, 84) \
  W(7) M(8, 64, 88) W(6) M(8, 68, 92) \
  W(5) M(12, 64, 96) RB(64, 120, 1024) W(5) M(12, 68, 100) RB(68, 121, 1024) \
  W(5) M(16, 72, 112) W(2) NEG M(16, 76, 116) \
  M(20, 72, 80) XA(1, 0) M(20, 76, 84) XB(1, 0) \
  M(24, 72, 88) XA(2, 1) M(24, 76, 92) XB(2, 1) \
  M(28, 72, 96) RB(72, 120, 1536) XA(3, 2) M(28, 76, 100) RB(76, 121, 1536) XB(3, 2) \
  W(3) M(32, 64, 112) W(2) M(32, 68, 116) \
  M(36, 64, 80) XA(16, 3) M(36, 68, 84) XB(16, 3) \
  M(40, 64, 88) XA(17, 16) M(40, 68, 92) XB(17, 16) \
  M(44, 64, 96) XA(18, 17) M(44, 68, 100) XB(18, 17) \
  W(1) M(48, 72, 112) XA(19, 18) W(0) M(48, 76, 116) SIG XB(19, 18) \
  M(52, 72, 80) L16 XA(32, 19) M(52, 76, 84) XB(32, 19) \
  M(56, 72, 88) XA(33, 32) M(56, 76, 92) XB(33, 32) \
  M(60, 72, 96) XA(34, 33) M(60, 76, 100) XB(34, 33) \
  XA(35, 34) XB(35, 34) "s_nop 3\n\t" XA(48, 35) XB(48, 35) \
  XA(49, 48) XB(49, 48) XA(50, 49) XB(50, 49) \
  XA(51, 50) XB(51, 50) W(0)
#define V2(op, d, a, b) op " v[" #d "], " a ", v[" #b "]\n\t"
#define CV(d, a) "v_cvt_pk_bf16_f32 v[" #d "], v[" #a "], v[" #a "+4]\n\t"
#define ST(d, inc, pol) "buffer_store_dwordx4 v[" #d ":" #d "+3], v123, %[rs], %[sf] offen " pol "\n\ts_add_u32 %[sf], %[sf], " #inc "\n\t"
#define C4(d, a) CV(d, a) CV(d + 1, a + 8) CV(d + 2, a + 1) CV(d + 3, a + 9)
#define SP(d) ST(d, 1024, "sc1")
#define UN(d, p) V2("v_lshlrev_b32", d, "16", p) V2("v_and_b32", d + 1, "0xffff0000", p) V2("v_lshlrev_b32", d + 2, "16", p + 1) V2("v_and_b32", d + 3, "0xffff0000", p + 1)
#define A1(d, a) "v_add_f32 v[" #d "], v[" #a "], v[" #d "]\n\t"
#define AS(d, a, inc) A1(d, a) A1(d + 1, a + 4) A1(d + 2, a + 8) A1(d + 3, a + 12) ST(d, inc, "nt")
#define LE1 "v_lshlrev_b32 v123, 4, v108\n\t"
#define LL1 "v_lshl_add_u32 v122, v108, 4, %[pp]\n\tv_and_b32 v123, 15, v108\n\t"
#define LL2 "v_lshrrev_b32 v108, 4, v108\n\tv_lshlrev_b32 v108, 15, v108\n\tv_lshl_add_u32 v123, v123, 4, v108\n\t"
#define PLW "v_mov_b32 v104, 0\n6:\n\tds_read_b32 v105, v104 offset:%[e]\n\t" W(0) "v_readfirstlane_b32 %[t], v105\n\ts_cmp_lt_u32 %[t], 16\n\ts_cbranch_scc1 6b\n\t"
#define TAIL1E \
  RT(64, 120, 4096) RT(66, 121, 4096) RT(68, 120, 4608) RT(70, 121, 4608) M(16, 72, 112) LA0 LE1 M(16, 76, 116) \
  M(20, 72, 80) M(20, 76, 84) \
  M(24, 72, 88) M(24, 76, 92) C4(104, 0) SP(104) \
  M(28, 72, 96) RT(72, 120, 6144) M(28, 76, 100) RT(74, 121, 6144) C4(108, 2) SP(108) \
  W(4) M(32, 64, 112) RT(76, 120, 6656) W(3) M(32, 68, 116) RT(78, 121, 6656) \
  M(36, 64, 80) M(36, 68, 84) \
  M(40, 64, 88) M(40, 68, 92) C4(0, 16) SP(0) \
  M(44, 64, 96) M(44, 68, 100) C4(4, 18) SP(4) \
  W(2) M(48, 72, 112) W(0) M(48, 76, 116) \
  M(52, 72, 80) M(52, 76, 84) \
  M(56, 72, 88) M(56, 76, 92) C4(8, 32) SP(8) \
  M(60, 72, 96) M(60, 76, 100) C4(12, 34) SP(12) \
  "s_nop 7\n\t" C4(16, 48) C4(20, 50) \
  SP(16) SP(20)
#define TAIL1L \
  RT(64, 120, 4096) RT(66, 121, 4096) RT(68, 120, 4608) RT(70, 121, 4608) M(16, 72, 112) LA0 LL1 M(16, 76, 116) LL2 \
  M(20, 72, 80) PLW RB(104, 122, 0) M(20, 76, 84) \
  M(24, 72, 88) W(0) UN(108, 104) M(24, 76, 92) AS(108, 0, 8192) \
  M(28, 72, 96) RT(72, 120, 6144) RB(108, 122, 1024) UN(104, 106) M(28, 76, 100) RT(74, 121, 6144) AS(104, 1, 8192) \
  M(32, 64, 112) RT(76, 120, 6656) W(2) UN(104, 108) M(32, 68, 116) RT(78, 121, 6656) AS(104, 2, 8192) \
  M(36, 64, 80) RB(104, 122, 2048) UN(108, 110) M(36, 68, 84) AS(108, 3, 106496) \
  M(40, 64, 88) RB(108, 122, 3072) M(40, 68, 92) W(1) UN(0, 104) \
  M(44, 64, 96) AS(0, 16, 8192) M(44, 68, 100) UN(4, 106) \
  M(48, 72, 112) AS(4, 17, 8192) RB(104, 122, 4096) M(48, 76, 116) W(1) UN(8, 108) \
  M(52, 72, 80) AS(8, 18, 8192) M(52, 76, 84) UN(12, 110) \
  M(56, 72, 88) AS(12, 19, 106496) RB(108, 122, 5120) M(56, 76, 92) W(1) UN(16, 104) \
  M(60, 72, 96) AS(16, 32, 8192) M(60, 76, 100) UN(20, 106) \
  AS(20, 33, 8192) RB(104, 122, 6144) W(1) UN(24, 108) AS(24, 34, 8192) UN(28, 110) \
  AS(28, 35, 106496) RB(108, 122, 7168) "s_nop 7\n\t" W(1) UN(32, 104) AS(32, 48, 8192) UN(36, 106) \
  AS(36, 49, 8192) W(0) UN(40, 108) AS(40, 50, 8192) UN(44, 110) \
  AS(44, 51, 106496)
#define ASK "v_mov_b32 v112, 0\n\tds_read_b32 v112, v112 offset:%[o]\n\t"
#define CHK "v_readfirstlane_b32 %[t], v112\n\ts_cmp_lt_u32 %[t], %[cnt]\n\t"
#define LOOP(X)                                                                                                           \
  "1:\n\t" STEP##X(104, 112, , , , "s_barrier\n\t") "5:\n\t"                                                                \
  STEP##X(112, 104, , , "s_cmp_lg_u32 %[k], %[k1]\n\t", "s_cbranch_scc0 2f\n\ts_barrier\n2:\n\t") "s_cbranch_scc1 1b\n\t" \
  STEP##X(104, 112, ASK, CHK "s_cbranch_scc0 4f\n3:\n\t" ASK W(0) CHK "s_cbranch_scc1 3b\n4:\n\t", , )
#define ACC \
  "+{v[0:3]}"(acc[0][0]), "+{v[4:7]}"(acc[0][1]), "+{v[8:11]}"(acc[0][2]), "+{v[12:15]}"(acc[0][3]), \
  "+{v[16:19]}"(acc[1][0]), "+{v[20:23]}"(acc[1][1]), "+{v[24:27]}"(acc[1][2]), "+{v[28:31]}"(acc[1][3]), \
  "+{v[32:35]}"(acc[2][0]), "+{v[36:39]}"(acc[2][1]), "+{v[40:43]}"(acc[2][2]), "+{v[44:47]}"(acc[2][3]), \
  "+{v[48:51]}"(acc[3][0]), "+{v[52:55]}"(acc[3][1]), "+{v[56:59]}"(acc[3][2]), "+{v[60:63]}"(acc[3][3]), \
  "+{v120}"(pa0), "+{v121}"(pa1), "+{v122}"(pb0), "+{v123}"(pb1)
#define CLOB \
  "scc", "memory", "v64", "v65", "v66", "v67", "v68", "v69", "v70", "v71", "v72", "v73", "v74", "v75", "v76", "v77", "v78", "v79", \
  "v80", "v81", "v82", "v83", "v84", "v85", "v86", "v87", "v88", "v89", "v90", "v91", "v92", "v93", "v94", "v95", "v96", "v97", \
  "v98", "v99", "v100", "v101", "v102", "v103", "v104", "v105", "v106", "v107", "v108", "v109", "v110", "v111", "v112", "v113", \
  "v114", "v115", "v116", "v117", "v118", "v119"
__device__ __forceinline__ void run0(f32x4 (&acc)[4][4], uint32_t pa0, uint32_t pb0, uint32_t pp, u32x4& lam, u32x4& l16, u32x4& l32) {
  uint32_t pa1 = pa0 ^ 64, pb1 = pb0 ^ 64, t;
  int k = 0, adv;
  asm volatile("" : ACC);
  bar_lds();
  asm volatile(HEAD0(104) LOOP(0) TAIL0
               : ACC, [k] "+s"(k), [adv] "=&s"(adv), [t] "=&s"(t)
               : [k1] "n"(14), [m3] "s"(-3 * G0_STAGE), [cnt] "n"(16), [o] "n"(SW_LD * 4), [e] "n"(SW_G0E * 4 - 4), [pp] "s"(pp), [lo] "n"(L_PRM - L_PART)
               : CLOB);
  asm volatile("" : "={v[104:107]}"(lam), "={v[64:67]}"(l16), "={v[68:71]}"(l32));
  __builtin_amdgcn_sched_barrier(0);
}
__device__ __forceinline__ void run1(f32x4 (&acc)[4][4], uint32_t pa0, uint32_t pb0, int n, uint32_t pp, u32x4 rs, uint32_t sf) {
  uint32_t pa1 = pa0 ^ 64, pb1 = pb0 ^ 64, t;
  int k = 1, adv;
  asm volatile("" : ACC);
  bar_lds();
#define REST \
  LOOP(1) ROW1(112) "s_cmp_lg_u32 %[pp], 0\n\ts_cbranch_scc1 7f\n\t" TAIL1E "s_branch 8f\n7:\n\t" TAIL1L "8:" \
  : ACC, [k] "+s"(k), [adv] "=&s"(adv), [t] "=&s"(t), [sf] "+s"(sf) \
  : [k1] "s"(n - 1), [m3] "s"(-3 * G0_STAGE), [cnt] "n"(32), [o] "n"(SW_LD * 4), [e] "n"(SW_PL * 4), [pp] "s"(pp), [rs] "s"(rs) \
  : CLOB
  if (NX & 1) asm volatile(HEAD1(112) "s_branch 5f\n\t" REST);
  else asm volatile(HEAD1(104) REST);
}
#undef W
#undef M

__device__ __forceinline__ void mfma_role(const Args& A, char* smem, int wave, int lane) {
  char* const ws = A.ws;
  const Tile T = tile_map();
  const int tm = T.tm, tn = T.tn;
  lds_u32* const sw = (lds_u32*)(smem + L_SYNC);
  const uint32_t U = (uint32_t)(size_t)(sw + 1);
  const int wr = wave >> 1, wc = wave & 1;
  int fr = lane & 15, fq = lane >> 4;
  uint32_t hw;
  if (T.late) {
    uint32_t n[2] = {0, 0};
    auto probe = [&](int i) {
      const int h = (i ^ (i >> 1) ^ wave) & 1;
      u64 t0, t1;
      uint32_t v;
      asm volatile("s_memtime %0\n\ts_waitcnt lgkmcnt(0)\n\ts_load_dword %2, %3, 0\n\ts_waitcnt lgkmcnt(0)\n\ts_memtime %1\n\ts_waitcnt lgkmcnt(0)"
                   : "=&s"(t0), "=&s"(t1), "=&s"(v)
                   : "s"(ws + WS_PG + tm * (2 << 20) + (2 << 20) - 32768 + (T.nt >> 2) * 16384 + h * 8192 + (((T.nt & 3) * 3 + wave) * 5 + i) * 128)
                   : "memory");
      const uint32_t d = (uint32_t)(t1 - t0), g = d < 4096u ? 4096u - d : 0u;
      n[0] = !h && g > n[0] ? g : n[0], n[1] = h && g > n[1] ? g : n[1];
    };
    const u64 t0 = __builtin_amdgcn_s_memrealtime();
    bar_lds();
    if (wave < 3) {
#pragma nounroll
      for (int i = 0; i < 5 && __builtin_amdgcn_s_memrealtime() - t0 < 200; ++i) probe(i);
      if (lane < 2) __hip_atomic_fetch_max(sw + SW_N0 + lane, lane ? n[1] : n[0], __ATOMIC_RELAXED, __HIP_MEMORY_SCOPE_WORKGROUP);
    }
  }
  if (wave == 3) {
    wait_vm<0>();
    const u16* raw = reinterpret_cast<const u16*>(smem + L_PRM + 512);
    uint32_t pr = raw[lane], pi = raw[64 + lane], pd = raw[128 + lane];
    asm volatile("" : "+v"(pr), "+v"(pi), "+v"(pd)::"memory");
    auto expf_ = [](float x) {
      const float t = x * 1.44269504088896341f;
      const float r = __builtin_fmaf(x, 1.44269504088896341f, -t) + x * 1.92596299112661746e-8f;
      const float e = __builtin_amdgcn_exp2f(t);
      return __builtin_fmaf(e * r, 0.693147180559945309f, e);
    };
    const float lr = u2f(pr << 16), li = u2f(pi << 16);
    const float dt = rbf(expf_(u2f(pd << 16)));
    const float zr = dt * lr, zi = dt * li;
    const float e = expf_(zr);
    const float rh = zi * 0.159154936671257019f;
    const float rf = (rh - __builtin_rintf(rh)) + __builtin_fmaf(zi, 6.42063832653e-9f, __builtin_fmaf(zi, 0.159154936671257019f, -rh));
    const cf lam = cf{e * __builtin_amdgcn_cosf(rf), e * __builtin_amdgcn_sinf(rf)};
    const float nr = lam.x - 1.0f, ni = lam.y;
    const float den = 2.0f / (lr * lr + li * li);
    cf* prm = reinterpret_cast<cf*>(smem + L_PRM);
    prm[lane] = lam;
    prm[64 + lane] = cf{(nr * lr + ni * li) * den, (ni * lr - nr * li) * den};
    cf lp[10];
    lp[0] = cmul(lam, lam);
#pragma unroll
    for (int k = 1; k < 10; ++k) lp[k] = cmul(lp[k - 1], lp[k - 1]);
    cf* pt = reinterpret_cast<cf*>(smem + L_PART) + lane;
    pt[64] = lp[3], pt[128] = lp[4], pt[192] = lp[5], pt[256] = lp[0], pt[320] = lp[1], pt[384] = lp[6];
    pt = reinterpret_cast<cf*>(smem + L_GRP) + lane;
    pt[0] = cf{1.f, 0.f}, pt[64] = lp[8], pt[128] = lp[9], pt[192] = cmul(lp[8], lp[9]);
  }
  __builtin_amdgcn_s_setprio(1);
  {
    f32x4 acc[4][4];
#pragma unroll
    for (int m = 0; m < 4; ++m)
#pragma unroll
      for (int n = 0; n < 4; ++n) acc[m][n] = Z4;
    const cf* prm = reinterpret_cast<const cf*>(smem + L_PRM) + wc * 32;
    cf* part = reinterpret_cast<cf*>(smem + L_PART) + wc * 32;
    cf lam[2], l16[2], l32[2], s[2][16];
    const uint64_t ag = (uint64_t)(ws + WS_AGG) + ((2 * tm + wr) * 16 + tn) * 2048 + wc * 640;
    u32x4 ra = u32x4{(uint32_t)ag, (uint32_t)(ag >> 32) & 0xFFFFu, wc ? 512u : 640u, 0x00020000u};
    {
      u32x4 t0, t1, t2;
      asm volatile("" : "+s"(ra));
      run0(acc, lds_base(smem) + (wr * 64 + (fr >> 2) * 16 + (fr & 3)) * 128 + (fq ^ (fr & 7)) * 16,
           lds_base(smem) + (128 + wc * 64 + fr) * 128 + (fq ^ (fr & 7)) * 16, lds_base(smem) + L_PART + wc * 256, t0, t1, t2);
#pragma unroll
      for (int jp = 0; jp < 2; ++jp) {
        lam[jp] = cf{u2f(t0[2 * jp]), u2f(t0[2 * jp + 1])};
        l16[jp] = cf{u2f(t1[2 * jp]), u2f(t1[2 * jp + 1])};
        l32[jp] = cf{u2f(t2[2 * jp]), u2f(t2[2 * jp + 1])};
      }
      lane = lane_id();
      fr = lane & 15;
      fq = lane >> 4;
#pragma unroll
      for (int i = 0; i < 32; ++i) s[i >> 4][i & 15] = cf{acc[(i >> 2) & 3][2 * (i >> 4)][i & 3], acc[(i >> 2) & 3][2 * (i >> 4) + 1][i & 3]};
    }
    const uint64_t m1 = 0xFFFF0000FFFF0000ull, m2 = 0xFFFFFFFF00000000ull;
    cf pe[2], a01[2], wg[2];
#pragma unroll
    for (int jp = 0; jp < 2; ++jp) {
      const u32x2 ex = __builtin_amdgcn_permlane16_swap(f2u(s[jp][15].x), f2u(s[jp][15].x), false, false);
      const u32x2 ey = __builtin_amdgcn_permlane16_swap(f2u(s[jp][15].y), f2u(s[jp][15].y), false, false);
      pe[jp] = cf{u2f(ex[0]), u2f(ey[0])};
      const cf ap = cmad(pe[jp], l16[jp], cf{u2f(ex[1]), u2f(ey[1])});
      const u32x2 hx = __builtin_amdgcn_permlane32_swap(f2u(ap.x), f2u(ap.x), false, false);
      const u32x2 hy = __builtin_amdgcn_permlane32_swap(f2u(ap.y), f2u(ap.y), false, false);
      a01[jp] = cf{u2f(hx[0]), u2f(hy[0])};
      wg[jp] = cmad(a01[jp], l32[jp], cf{u2f(hx[1]), u2f(hy[1])});
    }
    {
      const cf ws_ = sel_c(m1, wg[1], wg[0]);
      asm volatile("buffer_store_dwordx4 %0, %1, %2, 0 offen sc1\n\ts_nop 1"
                   ::"v"(u32x4{f2u(ws_.x), 1u, f2u(ws_.y), 1u}), "v"(wc ? lane * 16 : agg_off(lane)), "s"(ra) : "memory");
      if (wr == 0) {
        part[lane & 31] = ws_;
        wait_lgkm0();
      }
      lds_signal<SW_PUB>(U);
      lds_wait<SW_PUB, 0>(U, 4);
    }
    lds_wait<SW_RS, 1>(U, 1);
    lds_wait<SW_RS, 1>(U, 1);
    hw = lds_peek<SW_HALF>(U);
    __builtin_amdgcn_s_setprio(0);
    cf l2[2], l3[2], l4[2], c[2], pw[2];
#pragma unroll
    for (int jp = 0; jp < 2; ++jp) {
      const cf cof = prm[64 + jp * 16 + fr];
      l2[jp] = part[256 + jp * 16 + fr];
      l4[jp] = part[320 + jp * 16 + fr];
      const cf p1 = sel_c(m1, l16[jp], cf{1.f, 0.f});
      cf cc = sel_c(m1, pe[jp], cf{0.f, 0.f});
      cc = sel_c(m2, cmad(p1, a01[jp], cc), cc);
      cf p = sel_c(m2, cmul(p1, l32[jp]), p1);
      if (wr) {
        cc = cmad(p, part[jp * 16 + fr], cc);
        p = cmul(p, part[192 + jp * 16 + fr]);
      }
      pw[jp] = cmul(p, cof);
      c[jp] = cmul(cc, cof);
      l3[jp] = cmul(l2[jp], lam[jp]);
      asm volatile("" : "+v"(pw[jp]), "+v"(c[jp]), "+v"(l3[jp]), "+v"(l4[jp]));
#pragma unroll
      for (int i = 0; i < 16; ++i) {
        s[jp][i] = cmul(s[jp][i], cof);
        asm volatile("" : "+v"(s[jp][i]));
      }
    }
    const uint64_t hs = (uint64_t)(ws + WS_PG) + tm * (2 << 20) + (wr * 32 + tn * 2) * 16384 + half_of(hw, tm, wr);
    u32x4 rs = u32x4{(uint32_t)hs, (uint32_t)(hs >> 32) & 0xFFFFu, 32768u, 0x00020000u};
    uint32_t hb[2], ho[2], ga = lds_base(smem) + L_GRP + (wc * 32 + fr) * 8;
#pragma unroll
    for (int h = 0; h < 2; ++h) hb[h] = (fq * 2048 + (wc * 32 + fr) * 16) ^ (h * 64), ho[h] = hb[h] + lds_base(smem) + G0_STAGE + wr * 8192;
    asm volatile("" : "+s"(rs), "+v"(hb[0]), "+v"(hb[1]), "+v"(ho[0]), "+v"(ho[1]), "+v"(ga));
    __builtin_amdgcn_s_setprio(3);
    lds_wait<SW_GRP, 1>(U, 4);
    typedef __attribute__((address_space(3))) cf lds_cf;
    cf t[2];
#pragma unroll
    for (int jp = 0; jp < 2; ++jp) {
      const lds_cf* g = (const lds_cf*)(size_t)ga + jp * 16;
      t[jp] = cmad(pw[jp], (g[192] + g[128]) + (g[64] + g[0]), c[jp]);
    }
    asm volatile("" : "+v"(t[0]), "+v"(t[1]));
#pragma unroll
    for (int jh = 0; jh < 4; ++jh) {
      const int jp = jh >> 1, h = jh & 1;
#pragma unroll
      for (int k = 2 * h; k < 2 * h + 2; ++k) {
        s[jp][4 * k] = cmad(t[jp], lam[jp], s[jp][4 * k]);
        s[jp][4 * k + 1] = cmad(t[jp], l2[jp], s[jp][4 * k + 1]);
        s[jp][4 * k + 2] = cmad(t[jp], l3[jp], s[jp][4 * k + 2]);
        t[jp] = cmul(t[jp], l4[jp]);
        s[jp][4 * k + 3] = s[jp][4 * k + 3] + t[jp];
      }
      u32x4 re, im;
#pragma unroll
      for (int i = 0; i < 4; ++i) {
        re[i] = pk_bf(s[jp][8 * h + 2 * i].x, s[jp][8 * h + 2 * i + 1].x);
        im[i] = pk_bf(-s[jp][8 * h + 2 * i].y, -s[jp][8 * h + 2 * i + 1].y);
      }
      asm volatile("buffer_store_dwordx4 %0, %2, %3, 0 offen offset:%4 sc1\n\tbuffer_store_dwordx4 %1, %2, %3, %5 offen offset:%4 sc1\n\ts_nop 1"
                   ::"v"(re), "v"(im), "v"(hb[h]), "s"(rs), "n"(jp * 256 + h * 1024), "s"(16384) : "memory");
      if (T.late) sta4(ho[h] + jp * 256 + h * 1024, re), sta4(ho[h] + jp * 256 + h * 1024 + G0_STAGE, im);
      __builtin_amdgcn_sched_barrier(0);
    }
  }
  lds_signal<SW_HS>(U);
  if (T.late) {
    __builtin_amdgcn_s_setprio(0);
    wait_lgkm0();
    bar();
    bar();
    publish(counter(ws, 4 * tm + 2), lane);
    bar();
    bar();
  } else {
    publish(counter(ws, 4 * tm + (T.nt >> 2)), lane);
    lds_signal<SW_HD>(U);
    if (wave == 0) poll_count<SW_G1>(counter(ws, 4 * tm + 1), 16, U);
  }
}

__device__ __forceinline__ void gemm1(const Args& A, char* smem, int wave, int lane) {
  char* const ws = A.ws;
  const Tile T = tile_map();
  const int tm = T.tm, nt = T.nt;
  const uint32_t U = 4;
  const int wr = wave >> 1, wc = wave & 1, fr = lane & 15, fq = lane >> 4;
  f32x4 acc[4][4];
  uint32_t hw;
  if (T.late) {
    const u32x2* px = reinterpret_cast<const u32x2*>(A.xB + (tm * 128 + wr * 64 + fq * 4) * 1024 + nt * 128 + wc * 64) + fr;
    const u32x2 dw = reinterpret_cast<const u32x2*>(A.Dsk + nt * 128 + wc * 64)[fr];
    u32x2 xw[16];
#pragma unroll
    for (int i = 0; i < 16; ++i) xw[i] = px[((i >> 2) * 16 + (i & 3)) * 256];
#pragma unroll
    for (int i = 0; i < 32; ++i) {
      const uint32_t a = xw[i >> 1][i & 1], d = dw[i & 1];
      const uint32_t p = pk_bf(u2f(a << 16) * u2f(d << 16), u2f(a & 0xFFFF0000u) * u2f(d & 0xFFFF0000u));
      acc[i >> 3][(i & 1) * 2][(i >> 1) & 3] = u2f(p << 16);
      acc[i >> 3][(i & 1) * 2 + 1][(i >> 1) & 3] = u2f(p & 0xFFFF0000u);
    }
#pragma unroll
    for (int m = 0; m < 4; ++m) asm volatile("" : "+v"(acc[m][0]), "+v"(acc[m][1]), "+v"(acc[m][2]), "+v"(acc[m][3]));
    lds_wait<SW_G0E, 2>(U, 4);
#pragma nounroll
    for (int k = 0; k < 3; ++k) {
      if (k == 2) lds_wait<SW_GRP, 2>(U, 4);
      stage_c(A, wave, lane, k);
    }
    wait_vm<0>();
  } else {
#pragma unroll
    for (int i = 0; i < 16; ++i) acc[i >> 2][i & 3] = Z4;
  }
  {
    hw = lds_peek<SW_HALF>(U);
    const uint64_t base = T.late ? (uint64_t)A.out : (uint64_t)ps_of(ws, hw, tm, nt, wave);
    __builtin_amdgcn_s_setprio(1);
    run1(acc, lds_base(smem) + G0_STAGE + wr * 8192 + ((fr >> 1) & 1) * 1024 + (8 * fq + (fr >> 2) + (fr & 2) * 2) * 16 + (fr & 1) * 8,
         lds_base(smem) + G0_STAGE + 16384 + (wc * 64 + fr) * 128 + (fq ^ (fr & 7)) * 16, T.late ? 32 - NX : NX,
         T.late ? lds_base(smem) + ((29 - NX) & 3) * G0_STAGE + wave * 8192 : 0,
         u32x4{(uint32_t)base, (uint32_t)(base >> 32) & 0xFFFFu, 16u << 20, 0x00020000u},
         T.late ? ((tm * 128 + wr * 64) * 2048 + 1024 + nt * 128 + wc * 64) * 4 : 0);
  }
  if (!T.late) {
    lane = lane_id();
    lds_signal<SW_PL>(U);
    publish(counter(ws, 64 + bid_x()), lane);
    warm_dev(smem, ps_of(ws, hw, tm, nt, wave), lane * 128);
  }
}

}

extern "C" __global__ __launch_bounds__(1024, 4) void k_all(const uint32_t* x2, const unsigned short* w_r, const unsigned short* w_i,
                                                           const unsigned short* xB, const unsigned short* Lre,
                                                           const unsigned short* Lim, const unsigned short* Bre,
                                                           const unsigned short* Bim, const unsigned short* Cre,
                                                           const unsigned short* Cim, const unsigned short* ldt,
                                                           const unsigned short* Dsk, float* out, char* ws) {
  __shared__ __attribute__((aligned(256))) char smem_[LDS_BYTES];
  char* const smem = smem_ + 256;
  const Args A{x2, w_r, w_i, xB, Lre, Lim, Bre, Bim, Cre, Cim, ldt, Dsk, out, ws};
  const int tid = tid_x(), lane = tid & 63;
  int wave = __builtin_amdgcn_readfirstlane(tid >> 6);
  const uint32_t ldsb = lds_base(smem);
  {
    const int o = lane * 128 + (lane >> 5) * ((bid_x() << 9) & 0x7000);
    uint32_t t0, t1, t2, t3;
    asm volatile(
        "s_cmp_ge_u32 %[w], 8\n\ts_cbranch_scc1 8f\n\t"
        "s_load_dwordx2 s[64:65], %[ka], 0\n\ts_load_dwordx2 s[66:67], %[ka], 24\n\ts_load_dwordx4 s[68:71], %[ka], 48\n\t"
        "s_cmp_lg_u32 %[w], 0\n\ts_cbranch_scc1 1f\n\ts_getpc_b64 s[72:73]\n\ts_add_u32 s72, s72, k_all@rel32@lo+4\n\t"
        "s_addc_u32 s73, s73, k_all@rel32@hi+12\n\ts_mov_b32 m0, %[scr]\n\ts_nop 0\n\tglobal_load_lds_dword %[o], s[72:73]\n"
        "1:\n\tv_lshrrev_b32 %0, 3, %[l]\n\tv_and_b32 %1, 7, %[l]\n\ts_lshl_b32 s72, %[w], 12\n\t"
        "s_cmp_lt_u32 %[b], 128\n\ts_cbranch_scc1 2f\n\t"
        "s_add_u32 m0, s72, %[s3]\n\ts_lshl_b32 s72, %[w], 19\n\ts_and_b32 s73, %[b], 7\n\ts_lshl_b32 s73, s73, 11\n\ts_add_u32 s72, s72, s73\n\t"
        "s_lshl_b32 s73, %[b], 4\n\ts_and_b32 s73, s73, 0x780\n\ts_add_u32 s72, s72, s73\n\t"
        "v_lshrrev_b32 %2, 1, %0\n\tv_xor_b32 %1, %1, %2\n\tv_lshlrev_b32 %1, 4, %1\n\tv_lshl_add_u32 %0, %0, 14, %1\n\t"
        "v_add_u32 %0, s72, %0\n\tv_xor_b32 %1, 64, %0\n\tv_add_u32 %1, 130048, %1\n\tv_add_u32 %2, 260096, %0\n\t"
        "v_add_u32 %3, 260096, %1\n\ts_waitcnt lgkmcnt(0)\n\t"
        "global_load_lds_dwordx4 %0, s[64:65] nt\n\tglobal_load_lds_dwordx4 %1, s[64:65] offset:1024 nt\n\t"
        "global_load_lds_dwordx4 %2, s[64:65] offset:2048 nt\n\tglobal_load_lds_dwordx4 %3, s[64:65] offset:3072 nt\n\ts_branch 9f\n"
        "2:\n\ts_add_u32 m0, s72, 256\n\ts_mul_i32 s73, %[b], 1280\n\ts_and_b32 s73, s73, 0x600\n\t"
        "s_cmp_ge_u32 %[w], 4\n\ts_cbranch_scc1 3f\n\t"
        "s_lshl_b32 s72, %[b], 17\n\ts_and_b32 s72, s72, 0x3c0000\n\ts_add_u32 s73, s73, s72\n\ts_lshl_b32 s72, %[w], 16\n\t"
        "s_add_u32 s73, s73, s72\n\tv_and_b32 %2, 3, %0\n\tv_xor_b32 %1, %1, %2\n\tv_lshlrev_b32 %1, 4, %1\n\t"
        "v_lshl_add_u32 %0, %0, 11, %1\n\tv_add_u32 %0, s73, %0\n\tv_add_u32 %1, 15360, %0\n\tv_xor_b32 %2, 64, %0\n\t"
        "v_add_u32 %3, 46080, %2\n\tv_add_u32 %2, 30720, %2\n\ts_waitcnt lgkmcnt(0)\n\t"
        "global_load_lds_dwordx4 %0, s[66:67]\n\tglobal_load_lds_dwordx4 %1, s[66:67] offset:1024\n\t"
        "global_load_lds_dwordx4 %2, s[66:67] offset:2048\n\tglobal_load_lds_dwordx4 %3, s[66:67] offset:3072\n\ts_branch 9f\n"
        "3:\n\ts_and_b32 s72, %[b], 1\n\ts_lshl_b32 s72, s72, 19\n\ts_add_u32 s73, s73, s72\n\ts_lshl_b32 s72, %[b], 12\n\t"
        "s_and_b32 s72, s72, 0x60000\n\ts_add_u32 s73, s73, s72\n\ts_and_b32 s72, %[w], 3\n\ts_lshl_b32 s72, s72, 15\n\t"
        "s_add_u32 s73, s73, s72\n\tv_xor_b32 %1, %1, %0\n\tv_lshlrev_b32 %1, 4, %1\n\tv_lshl_add_u32 %0, %0, 11, %1\n\t"
        "v_add_u32 %0, s73, %0\n\tv_add_u32 %1, 15360, %0\n\ts_waitcnt lgkmcnt(0)\n\t"
        "global_load_lds_dwordx4 %0, s[68:69]\n\tglobal_load_lds_dwordx4 %1, s[68:69] offset:1024\n\ts_add_u32 m0, m0, 2048\n\ts_nop 0\n\t"
        "global_load_lds_dwordx4 %0, s[70:71]\n\tglobal_load_lds_dwordx4 %1, s[70:71] offset:1024\n\ts_branch 9f\n"
        "8:\n\ts_cmp_lg_u32 %[w], 15\n\ts_cbranch_scc1 9f\n\ts_setprio 3\n\t"
        "s_load_dwordx4 s[64:67], %[ka], 32\n\ts_load_dwordx2 s[68:69], %[ka], 80\n\t"
        "s_and_b32 s72, %[b], 1\n\ts_lshl_b32 s72, s72, 9\n\ts_lshl_b32 s73, %[b], 2\n\ts_and_b32 s73, s73, 0x180\n\ts_add_u32 s72, s72, s73\n\t"
        "s_and_b32 s73, %[b], 128\n\ts_lshl_b32 s73, s73, 3\n\ts_add_u32 s72, s72, s73\n\tv_lshl_add_u32 %0, %[l], 2, s72\n\t"
        "s_mov_b32 m0, %[prm]\n\ts_mov_b32 exec_hi, 0\n\ts_waitcnt lgkmcnt(0)\n\tglobal_load_lds_dword %0, s[64:65]\n\t"
        "s_add_u32 m0, m0, 128\n\ts_nop 0\n\tglobal_load_lds_dword %0, s[66:67]\n\ts_add_u32 m0, m0, 128\n\ts_nop 0\n\t"
        "global_load_lds_dword %0, s[68:69]\n\ts_mov_b32 exec_hi, -1\n\ts_setprio 0\n9:"
        : "=&v"(t0), "=&v"(t1), "=&v"(t2), "=&v"(t3)
        : [l] "v"(lane), [o] "v"(o < 36672 - 16 ? o : 36672 - 16), [w] "s"(wave), [b] "s"(bid_x()),
          [ka] "s"(__builtin_amdgcn_kernarg_segment_ptr()), [scr] "n"(256 + L_SCR2), [s3] "n"(256 + 3 * G0_STAGE), [prm] "n"(256 + L_PRM + 512)
        : "s64", "s65", "s66", "s67", "s68", "s69", "s70", "s71", "s72", "s73", "scc", "memory");
  }
  if (wave < 8) {
  } else if (wave < 12) {
    const int wc = (bid_x() >> 3) & 15;
    if (bid_x() >= 128 && (wc & 7) != 3 && wave != 10)
      warm_r(A, smem, lane, wave == 11 ? (wc & 8) | 3 : wc, wave == 11 ? (wc & 7) - ((wc & 7) > 3) : wave - 8);
  } else if (wave == 13) {
    warm1(smem, Dsk, (lane & 15) * 128);
  }
  asm volatile("" : "+s"(wave));
  bool g1 = false;
  if (__builtin_expect(wave >= 12, 0)) {
    if (wave == 12) *(volatile lds_u32*)((lds_u32*)(smem + L_SYNC) + lane) = 0;
    mfma_role(A, smem, wave - 12, lane);
    g1 = bid_x() < 128;
  } else if (__builtin_expect(wave >= 8, 0)) {
    loader_role(A, smem, wave - 8, lane);
  } else {
    g1 = fft_role(A, smem, wave, lane);
  }
  if (g1) gemm1(A, smem, wave & 3, (int)__builtin_amdgcn_mbcnt_hi(~0u, __builtin_amdgcn_mbcnt_lo(~0u, 0u)));
}
#if defined(__HIP_DEVICE_COMPILE__) && __HIP_DEVICE_COMPILE__
#pragma clang attribute pop
#endif

extern "C" void kernel_launch(void* const* d_in, const int* in_sizes, int n_in, void* d_out, int out_size, void* d_ws,
                              size_t ws_size, hipStream_t stream) {
  typedef const unsigned short* P;
  k_all<<<256, 1024, 0, stream>>>((const uint32_t*)d_in[0], (P)d_in[1], (P)d_in[2], (P)d_in[3], (P)d_in[4], (P)d_in[5], (P)d_in[6],
                                  (P)d_in[7], (P)d_in[8], (P)d_in[9], (P)d_in[10], (P)d_in[11], (float*)d_out, (char*)d_ws);
}
```
